```python
import torch

def _softmax(x, axis=-1):
    return torch.softmax(x, dim=axis)
def _gelu(x):
    return torch.nn.functional.gelu(x, approximate="tanh")
def _silu(x):
    return torch.nn.functional.silu(x)
def _sigmoid(x):
    return torch.sigmoid(x)
def _layernorm(x, axis=-1, eps=1e-5):
    m = x.mean(dim=axis, keepdim=True)
    v = x.var(dim=axis, keepdim=True, unbiased=False)
    return (x - m) / torch.sqrt(v + eps)
def _scatter_add(tgt, idx, src, axis=0):
    return tgt.clone().index_add_(axis, idx, src)
def _segment_sum(src, seg, n):
    out = torch.zeros((n,) + src.shape[1:], dtype=src.dtype)
    return out.index_add_(0, seg, src)
def _pad(x, pw):
    flat = tuple(v for pair in reversed(pw) for v in pair)
    return torch.nn.functional.pad(x, flat)

kernel_name = 'concat_heads__ssm_conv_s4__ssm_conv_s5__prompt'

def setup_inputs():

    _w = (lambda a: (lambda t: t.to(torch.bfloat16) if t.is_floating_point() else t)(torch.as_tensor(a)))
    def _A_setup():
        B, L, D, N = 1, 2048, 576, 128
        A_re = -(lambda _x: torch.exp(torch.as_tensor(_x)))((lambda *s: torch.randn(*s, dtype=torch.bfloat16) * 3.2)(D, N))
        A_im = torch.pi * (lambda *s: torch.randn(*s, dtype=torch.bfloat16) * 0.05)(D, N)
        B_re = (lambda *s: torch.randn(*s, dtype=torch.bfloat16) * 0.1)(D, N)
        B_im = (lambda *s: torch.randn(*s, dtype=torch.bfloat16) * 0.1)(D, N)
        C_re = (lambda *s: torch.randn(*s, dtype=torch.bfloat16) * 0.1)(D, N)
        C_im = (lambda *s: torch.randn(*s, dtype=torch.bfloat16) * 0.1)(D, N)
        log_dt = (lambda *s: torch.randn(*s, dtype=torch.bfloat16) * 0.05)(D) * 0.5 - 4.0
        Dskip = (lambda *s: torch.randn(*s, dtype=torch.bfloat16) * 0.05)(D)
        return {
            "u": _w((lambda *s: torch.randn(*s, dtype=torch.bfloat16) * 0.05)(B, L, D)),
            "A_re": _w(A_re),
            "A_im": _w(A_im),
            "B_re": _w(B_re),
            "B_im": _w(B_im),
            "C_re": _w(C_re),
            "C_im": _w(C_im),
            "log_dt": _w(log_dt),
            "Dskip": _w(Dskip),
        }
    _A_in = _A_setup()

    def _B_setup():
        B, S, D = 1, 2048, 576
        P = D
        Lambda_re = -0.5 * (lambda _x: torch.exp(torch.as_tensor(_x)))((lambda *s: torch.randn(*s, dtype=torch.bfloat16) * 0.2)(P))
        Lambda_im = (lambda *s: torch.randn(*s, dtype=torch.bfloat16) * 0.4)(P) * 1.0
        B_re = (lambda *s: torch.randn(*s, dtype=torch.bfloat16) * 0.2)(P, D) * (D ** -0.5)
        B_im = (lambda *s: torch.randn(*s, dtype=torch.bfloat16) * 0.2)(P, D) * (D ** -0.5)
        C_re = (lambda *s: torch.randn(*s, dtype=torch.bfloat16) * 0.2)(D, P) * (P ** -0.5)
        C_im = (lambda *s: torch.randn(*s, dtype=torch.bfloat16) * 0.2)(D, P) * (P ** -0.5)
        log_dt = (lambda *s: torch.randn(*s, dtype=torch.bfloat16) * 0.2)(P) * 0.3 - 2.0
        Dskip = (lambda *s: torch.randn(*s, dtype=torch.bfloat16) * 0.05)(D)
        return {
            "x": _w((lambda *s: torch.randn(*s, dtype=torch.bfloat16) * 0.05)(B, S, D)),
            "Lambda_re": _w(Lambda_re),
            "Lambda_im": _w(Lambda_im),
            "B_re": _w(B_re),
            "B_im": _w(B_im),
            "C_re": _w(C_re),
            "C_im": _w(C_im),
            "log_dt": _w(log_dt),
            "Dskip": _w(Dskip),
        }
    _B_in = _B_setup()

    return {
        "A_u": _A_in["u"],
        "A_A_re": _A_in["A_re"],
        "A_A_im": _A_in["A_im"],
        "A_B_re": _A_in["B_re"],
        "A_B_im": _A_in["B_im"],
        "A_C_re": _A_in["C_re"],
        "A_C_im": _A_in["C_im"],
        "A_log_dt": _A_in["log_dt"],
        "A_Dskip": _A_in["Dskip"],
        "B_x": _B_in["x"],
        "B_Lambda_re": _B_in["Lambda_re"],
        "B_Lambda_im": _B_in["Lambda_im"],
        "B_B_re": _B_in["B_re"],
        "B_B_im": _B_in["B_im"],
        "B_C_re": _B_in["C_re"],
        "B_C_im": _B_in["C_im"],
        "B_log_dt": _B_in["log_dt"],
        "B_Dskip": _B_in["Dskip"],
    }

def reference(A_u, A_A_re, A_A_im, A_B_re, A_B_im, A_C_re, A_C_im, A_log_dt, A_Dskip, B_x, B_Lambda_re, B_Lambda_im, B_B_re, B_B_im, B_C_re, B_C_im, B_log_dt, B_Dskip):
    def _A_ref(u, A_re, A_im, B_re, B_im, C_re, C_im, log_dt, Dskip):
        Bsz, L, D = u.shape
        dt = torch.exp(log_dt)[:, None]
        A = A_re + 1j * A_im
        Bc = B_re + 1j * B_im
        Cc = C_re + 1j * C_im
        logdA = dt * A
        dB = dt * Bc
        l = (lambda x, dt: x.to(dt))(torch.arange(L), A_re.dtype)
        P = torch.exp(logdA[:, :, None] * l[None, None, :])
        K = torch.einsum("dn,dnl,dn->dl", Cc, P, dB).real
        L2 = 2 * L
        Kp = _pad(K, ((0, 0), (0, L)))
        up = (lambda x, *d: x.permute(*d))(u, 0, 2, 1)
        up = _pad(up, ((0, 0), (0, 0), (0, L)))
        Kf = (lambda x, axes=(-2,-1): torch.fft.rfft2(x.float(), dim=axes))(Kp[:, None, :], axes=(-2, -1))
        Uf = (lambda x, axes=(-2,-1): torch.fft.rfft2(x.float(), dim=axes))(up[:, :, None, :], axes=(-2, -1))
        Yf = Uf * Kf[None, :, :, :]
        y = (lambda x, s, axes=(-2,-1): torch.fft.irfft2(x, s=s, dim=axes).to(torch.bfloat16))(Yf, (1, L2), axes=(-2, -1))[:, :, 0, :L]
        y = (lambda x, *d: x.permute(*d))(y, 0, 2, 1) + u * Dskip[None, None, :]
        return y

    def _B_ref(x, Lambda_re, Lambda_im, B_re, B_im, C_re, C_im, log_dt, Dskip):
        Bsz, L, H = x.shape
        Lam = Lambda_re + 1j * Lambda_im
        Bmat = B_re + 1j * B_im
        Cmat = C_re + 1j * C_im
        dt = torch.exp(log_dt)
        Lam_bar = torch.exp(dt * Lam)
        B_bar = ((Lam_bar - 1.0) / Lam)[:, None] * Bmat
        Bu = torch.einsum("bld,pd->blp", x + 0j, B_bar)
        h = torch.zeros_like(Bu[:, 0, :])
        hs = []
        for t in range(L):
            h = Lam_bar[None, :] * h + Bu[:, t, :]
            hs.append(h)
        hseq = torch.stack(hs, 1)
        y = 2.0 * torch.einsum("blp,dp->bld", hseq, Cmat).real + x * Dskip[None, None, :]
        return y

    a = _A_ref(A_u, A_A_re, A_A_im, A_B_re, A_B_im, A_C_re, A_C_im, A_log_dt, A_Dskip)
    b = _B_ref(B_x, B_Lambda_re, B_Lambda_im, B_B_re, B_B_im, B_C_re, B_C_im, B_log_dt, B_Dskip)
    return torch.concatenate([a, b], axis=-1)
```

```cpp
#include <hip/hip_runtime.h>
#include <stdint.h>

#define LAS __attribute__((address_space(3)))
#define GAS __attribute__((address_space(1)))
typedef short bf16x8 __attribute__((ext_vector_type(8)));
typedef short s16x4 __attribute__((ext_vector_type(4)));
typedef float f32x2 __attribute__((ext_vector_type(2)));
typedef float f32x4 __attribute__((ext_vector_type(4)));
typedef float f32x16 __attribute__((ext_vector_type(16)));
typedef unsigned u32x2 __attribute__((ext_vector_type(2)));
typedef unsigned u32x4 __attribute__((ext_vector_type(4)));
typedef __bf16 bf2 __attribute__((ext_vector_type(2)));
typedef unsigned long long u64;
typedef unsigned short u16;
typedef LAS char* lptr;
#define MA __attribute__((may_alias))
typedef bf16x8 MA a_bf16x8; typedef s16x4 MA a_s16x4; typedef f32x2 MA a_f32x2; typedef f32x4 MA a_f32x4;
typedef u32x2 MA a_u32x2; typedef u32x4 MA a_u32x4; typedef unsigned MA a_u32; typedef u16 MA a_u16;

#define MFMA16(a, b, c) __builtin_amdgcn_mfma_f32_16x16x32_bf16(a, b, c, 0, 0, 0)
#define MFMA32(a, b, c) __builtin_amdgcn_mfma_f32_32x32x16_bf16(a, b, c, 0, 0, 0)

#define STAMP(k) do { } while (0)
#define FLUSH(wg, r, n) do { } while (0)

constexpr int L = 2048, D = 576, ROWB = D * 2;
constexpr size_t WS_H = 0, WS_GRAN = 8u << 20, WS_EPOCH = 9u << 20, WS_STAMP = 200u << 20;

struct Params {
  const u16* u; const u16* Are; const u16* Aim; const u16* Bre_a; const u16* Bim_a; const u16* Cre_a; const u16* Cim_a;
  const u16* ldt_a; const u16* dsk_a;
  const u16* x; const u16* Lre; const u16* Lim; const u16* Bre; const u16* Bim; const u16* Cre; const u16* Cim;
  const u16* ldt_b; const u16* dsk_b;
  float* out; char* ws;
};

__device__ __forceinline__ unsigned cvtpk(float a, float b) {
  f32x2 v = {a, b};
  return __builtin_bit_cast(unsigned, __builtin_convertvector(v, bf2));
}
__device__ __forceinline__ float asf(unsigned v) { return __builtin_bit_cast(float, v); }
__device__ __forceinline__ unsigned asu(float v) { return __builtin_bit_cast(unsigned, v); }
__device__ __forceinline__ float bflo(unsigned v) { return __builtin_bit_cast(float, v << 16); }
__device__ __forceinline__ float bfhi(unsigned v) { return __builtin_bit_cast(float, v & 0xffff0000u); }
__device__ __forceinline__ float rnd_bf16(float v) { return bflo(cvtpk(v, v)); }
struct CM { f32x2 r, i; };
__device__ __forceinline__ CM mkcm(f32x2 e) { CM m; m.r = e.xx; m.i = f32x2{-e.y, e.y}; return m; }
__device__ __forceinline__ f32x2 cmul(f32x2 a, const CM& m) { return __builtin_elementwise_fma(m.i, a.yx, m.r * a); }
__device__ __forceinline__ f32x2 cfma(f32x2 a, const CM& m, f32x2 b) {
  return __builtin_elementwise_fma(m.i, a.yx, __builtin_elementwise_fma(m.r, a, b));
}
__device__ __forceinline__ f32x2 cmul2(f32x2 a, f32x2 e) { return cmul(a, mkcm(e)); }
__device__ __forceinline__ f32x2 cexp_(float zr, float zi) {
  float m = __builtin_amdgcn_exp2f(zr * 1.4426950408889634f);
  float th = __builtin_amdgcn_fractf(zi * 0.15915494309189535f);
  return f32x2{m * __builtin_amdgcn_cosf(th), m * __builtin_amdgcn_sinf(th)};
}
__device__ __forceinline__ void glds16(const void* sbase, unsigned voff, unsigned dst) {
  asm volatile("s_mov_b32 m0, %2\n\ts_nop 0\n\tglobal_load_lds_dwordx4 %0, %1" ::"v"(voff), "s"(sbase), "s"(dst) : "memory");
}
#define WAITV(n) asm volatile("s_waitcnt vmcnt(" #n ")" ::: "memory")
#define BAR() asm volatile("s_waitcnt lgkmcnt(0)\n\ts_barrier" ::: "memory")
template <int N> __device__ __forceinline__ void waitv_bar() { asm volatile("s_waitcnt vmcnt(%0) lgkmcnt(0)\n\ts_barrier" ::"i"(N) : "memory"); }
__device__ __forceinline__ bf16x8 lds128(lptr p) { return *(LAS a_bf16x8*)p; }
__device__ __forceinline__ s16x4 ldstr(lptr p) { return __builtin_amdgcn_ds_read_tr16_b64_v4i16((LAS s16x4*)p); }
__device__ __forceinline__ bf16x8 cat(u32x2 a, u32x2 b) { u32x4 v = {a.x, a.y, b.x, b.y}; return __builtin_bit_cast(bf16x8, v); }
__device__ __forceinline__ bf16x8 cat4(unsigned a, unsigned b, unsigned c, unsigned d) { u32x4 v = {a, b, c, d}; return __builtin_bit_cast(bf16x8, v); }

constexpr int A_REG = 15232;
constexpr int A_STG = 8 * A_REG;
constexpr int A_YS = 4128;
constexpr int A_TROW = 272;
constexpr int A_R1 = 2048, A_UT = 4096;
constexpr int SQ_N = 0, SQ_M = 560, SQ_F = 560 + 4 * 64;

__device__ __forceinline__ u32x2 ld2(lptr p) { u32x2 r; r.x = *(LAS a_u32*)p; r.y = *(LAS a_u32*)(p + 4); return r; }

__device__ __forceinline__ bf16x8 band(int lane, int ctr, int h) {
  int base = (lane & 15) + ctr - 8 * (lane >> 4);
  unsigned w[4];
#pragma unroll
  for (int j = 0; j < 4; ++j) {
    int d0 = base - 2 * j, d1 = d0 - 1;
    unsigned lo = ((unsigned)(d0 + h) <= (unsigned)(2 * h)) ? 0x3f80u : 0u;
    unsigned hi = ((unsigned)(d1 + h) <= (unsigned)(2 * h)) ? 0x3f800000u : 0u;
    w[j] = lo | hi;
  }
  return cat4(w[0], w[1], w[2], w[3]);
}

__device__ __forceinline__ void partA(const Params& P, int cg, lptr lds, u64* stamps) {
  const int tid = threadIdx.x, lane = tid & 63, wid = __builtin_amdgcn_readfirstlane(tid >> 6);
  const int ch = cg * 8 + wid;
  const unsigned lds0 = (unsigned)(uintptr_t)lds;
  unsigned pa[6];
  {
    unsigned off = (unsigned)(ch * 256 + lane * 4);
    asm volatile("global_load_dword %0, %1, %2" : "=v"(pa[0]) : "v"(off), "s"(P.Are) : "memory");
    asm volatile("global_load_dword %0, %1, %2" : "=v"(pa[1]) : "v"(off), "s"(P.Aim) : "memory");
    asm volatile("global_load_dword %0, %1, %2" : "=v"(pa[2]) : "v"(off), "s"(P.Bre_a) : "memory");
    asm volatile("global_load_dword %0, %1, %2" : "=v"(pa[3]) : "v"(off), "s"(P.Bim_a) : "memory");
    asm volatile("global_load_dword %0, %1, %2" : "=v"(pa[4]) : "v"(off), "s"(P.Cre_a) : "memory");
    asm volatile("global_load_dword %0, %1, %2" : "=v"(pa[5]) : "v"(off), "s"(P.Cim_a) : "memory");
  }
#pragma unroll
  for (int k = 0; k < 4; ++k)
    glds16((const char*)P.u + cg * 16, (unsigned)(((wid * 4 + k) * 64 + lane) * ROWB), lds0 + A_STG + (wid * 4 + k) * 1024);
  const unsigned ldw = ((const unsigned*)P.ldt_a)[ch >> 1], dsw = ((const unsigned*)P.dsk_a)[ch >> 1];
  const float dt = rnd_bf16(expf((ch & 1) ? bfhi(ldw) : bflo(ldw)));
  const float dskip = (ch & 1) ? bfhi(dsw) : bflo(dsw);
  asm volatile("s_waitcnt vmcnt(4)" : "+v"(pa[0]), "+v"(pa[1]), "+v"(pa[2]), "+v"(pa[3]), "+v"(pa[4]), "+v"(pa[5])::"memory");
  STAMP(1);
  lptr reg = lds + wid * A_REG;
  f32x16 kacc = {};
  {
    lptr wr = reg + lane * 4;
    const int r32 = lane & 31, hh = lane >> 5;
    lptr rdA = reg + (r32 < 24 ? r32 : r32 - 8) * A_TROW + hh * 16;
    lptr rdB = reg + (24 + r32) * A_TROW + hh * 16;
#pragma unroll
    for (int ps = 0; ps < 2; ++ps) {
      float ar = ps ? bfhi(pa[0]) : bflo(pa[0]), ai = ps ? bfhi(pa[1]) : bflo(pa[1]);
      float br = ps ? bfhi(pa[2]) : bflo(pa[2]), bi = ps ? bfhi(pa[3]) : bflo(pa[3]);
      float cr = ps ? bfhi(pa[4]) : bflo(pa[4]), ci = ps ? bfhi(pa[5]) : bflo(pa[5]);
      float zr = dt * ar, zi = dt * ai;
      f32x2 W = cmul2(f32x2{cr, ci}, f32x2{dt * br, dt * bi});
      f32x2 e1 = cexp_(zr, zi), e8 = cexp_(zr * 8.f, zi * 8.f), e256 = cexp_(zr * 256.f, zi * 256.f);
      CM c1 = mkcm(f32x2{e1.x, -e1.y}), c8 = mkcm(f32x2{e8.x, -e8.y}), c256 = mkcm(f32x2{e256.x, -e256.y});
      auto put = [&](int row, f32x2 v) { *(LAS a_u32*)(wr + row * A_TROW) = cvtpk(v.x, v.y); };
      f32x2 p = f32x2{W.x, -W.y};
      f32x2 q256 = cmul(p, c256), q260;
      put(0, p); put(8, q256);
#pragma unroll
      for (int a = 1; a < 8; ++a) {
        p = cmul(p, c1); put(a, p);
        if ((a & 1) == 0) { f32x2 t = cmul(p, c256); put(8 + a / 2, t); if (a == 4) q260 = t; }
      }
      f32x2 q264 = cmul(q256, c8); put(16, q264);
      f32x2 q516 = cmul(q260, c256), q520 = cmul(q264, c256); put(12, q516); put(13, q520);
      f32x2 q772 = cmul(q516, c256), q = cmul(q520, c256); put(14, q772); put(15, q);
#pragma unroll
      for (int k = 0; k < 4; ++k) { q = cmul(q, c256); put(20 + k, q); }
      CM m8 = mkcm(e8);
      f32x2 e16 = cmul(e8, m8);
      CM m16 = mkcm(e16);
      f32x2 ga = f32x2{1.f, 0.f}, gb = e8;
      put(24, ga); put(25, gb);
      ga = e16; gb = cmul(gb, m16); put(26, ga); put(27, gb);
#pragma unroll
      for (int c = 4; c < 32; c += 2) { ga = cmul(ga, m16); gb = cmul(gb, m16); put(24 + c, ga); put(25 + c, gb); }
#pragma unroll
      for (int ks = 0; ks < 8; ++ks) kacc = MFMA32(lds128(rdA + ks * 32), lds128(rdB + ks * 32), kacc);
    }
  }
  STAMP(2);
  float k1024;
  {
    const int c = lane & 31, hh = lane >> 5;
    u32x4 z = {0u, 0u, 0u, 0u};
    *(LAS a_u32x4*)(reg + lane * 16) = z;
    *(LAS a_u32x4*)(reg + 1024 + lane * 16) = z;
    if (lane < 32) *(LAS a_u32x4*)(reg + A_UT + wid * 16 + lane * 16) = z;
    float k0 = kacc[0];
    if (lane == 0) k0 += dskip;
    u32x2 nw = {cvtpk(kacc[3], kacc[2]), cvtpk(kacc[1], k0)};
    *(LAS a_u32x2*)(reg + (SQ_N + 540 - 8 * c - 4 * hh) * 2) = nw;
    if (hh == 0) {
      u32x4 mw = {cvtpk(kacc[8], kacc[7]), cvtpk(kacc[6], kacc[6]), cvtpk(kacc[6], kacc[5]), cvtpk(kacc[4], kacc[4])};
      *(LAS a_u32x4*)(reg + (SQ_N + 280 - 8 * c) * 2) = mw;
      if (c == 31) {
        unsigned v = cvtpk(kacc[8], kacc[8]);
        *(LAS a_u32*)(reg + (SQ_N + 30) * 2) = v;
        *(LAS a_u16*)(reg + (SQ_N + 29) * 2) = (u16)v;
      }
    } else {
      const int odd = c & 1, cm = c >> 1;
      lptr m0 = reg + (SQ_M + (2 * odd) * 64 + 79 - 32 - cm) * 2;
      unsigned a = cvtpk(kacc[4], kacc[5]), b = cvtpk(kacc[6], kacc[7]);
      *(LAS a_u16*)(m0) = (u16)a; *(LAS a_u16*)(m0 + 128) = (u16)(a >> 16);
      *(LAS a_u16*)(m0 - 32) = (u16)b; *(LAS a_u16*)(m0 + 128 - 32) = (u16)(b >> 16);
      lptr f0 = reg + (SQ_F + odd * 80 + 127 - 64 - cm) * 2;
      unsigned e = cvtpk(kacc[8], kacc[9]), f = cvtpk(kacc[10], kacc[11]);
      *(LAS a_u16*)(f0) = (u16)e; *(LAS a_u16*)(f0 - 32) = (u16)(e >> 16);
      *(LAS a_u16*)(f0 - 64) = (u16)f; *(LAS a_u16*)(f0 - 96) = (u16)(f >> 16);
    }
    k1024 = asf(__builtin_amdgcn_readlane(asu(kacc[7]), 63));
#pragma unroll
    for (int it = 0; it < 2; ++it) {
      lptr s = reg + it * 1024 + lane * 16;
      u32x4 v = *(LAS a_u32x4*)s;
      unsigned nx = *(LAS a_u32*)(s + 16);
      u32x4 o = {__builtin_amdgcn_alignbit(v.y, v.x, 16), __builtin_amdgcn_alignbit(v.z, v.y, 16),
                 __builtin_amdgcn_alignbit(v.w, v.z, 16), __builtin_amdgcn_alignbit(nx, v.w, 16)};
      *(LAS a_u32x4*)(s + A_R1) = o;
    }
  }
  STAMP(3);
  waitv_bar<0>();
  STAMP(4);
  {
    const int gi = lane >> 4, q = (lane >> 2) & 3, pp = lane & 3, i16 = lane & 15;
    lptr src = lds + A_STG + (wid * 256 + gi * 8 + (pp >> 1) * 4 + q) * 16 + (pp & 1) * 8;
    lptr dst = lds + (i16 & 7) * (A_REG + 16) + A_UT + 512 + (wid * 256 + gi * 8 + (i16 >> 3) * 4) * 2;
#pragma unroll
    for (int k = 0; k < 8; ++k) *(LAS a_s16x4*)(dst + k * 64) = ldstr(src + k * 512);
  }
  BAR();
  STAMP(5);
  const int a16 = lane & 15, g4 = lane >> 4;
  lptr ub = reg + A_UT + wid * 16 + 512 + a16 * 32 + g4 * 16;
  lptr kn = reg + ((a16 & 1) ? (527 - a16 + 8 * g4) * 2 : A_R1 + (526 - a16 + 8 * g4) * 2);
  lptr kf = reg + ((a16 & 1) ? (79 - a16 + 4 * g4) * 2 : A_R1 + (78 - a16 + 4 * g4) * 2);
  f32x4 acc[8];
#pragma unroll
  for (int j = 0; j < 8; ++j) acc[j] = f32x4{0.f, 0.f, 0.f, 0.f};
  {
    const bf16x8 b12_1 = band(lane, 12, 1), b8_2 = band(lane, 8, 2), b4_1 = band(lane, 4, 1), b8_3 = band(lane, 8, 3), b8_4 = band(lane, 8, 4);
    bf16x8 KM[3][2], KF[4];
#pragma unroll
    for (int dl = 2; dl <= 4; ++dl)
#pragma unroll
      for (int pr = 0; pr < 2; ++pr)
        KM[dl - 2][pr] = cat(ld2(kf + (SQ_M + (2 * pr) * 64) * 2 - 32 * dl), ld2(kf + (SQ_M + (2 * pr + 1) * 64) * 2 - 32 * dl));
#pragma unroll
    for (int dl = 4; dl <= 7; ++dl) KF[dl - 4] = cat(ld2(kf + (SQ_F + 48) * 2 - 32 * dl), ld2(kf + (SQ_F + 80 + 48) * 2 - 32 * dl));
    bf16x8 KC;
    {
      unsigned kb = cvtpk(k1024, k1024), w[4];
      int j0 = a16 + 12 - 8 * g4;
#pragma unroll
      for (int j = 0; j < 4; ++j)
        w[j] = (((unsigned)(2 * j - j0) <= 1u) ? (kb & 0xffffu) : 0u) | (((unsigned)(2 * j + 1 - j0) <= 1u) ? (kb & 0xffff0000u) : 0u);
      KC = cat4(w[0], w[1], w[2], w[3]);
    }
    const f32x4 z4 = {0.f, 0.f, 0.f, 0.f};
#pragma unroll
    for (int i = 0; i < 6; ++i) {
      bf16x8 u16f = lds128(ub + 32 * (16 * i - 1)), u24f = lds128(ub + 32 * (16 * i - 1) - 16);
      f32x4 d4 = MFMA16(u16f, b12_1, z4), d8 = MFMA16(u16f, b8_2, z4), d12 = MFMA16(u16f, b4_1, z4), d16 = MFMA16(u24f, b8_2, z4);
      bf16x8 w0 = cat4(cvtpk(d4[0], d4[1]), cvtpk(d4[2], d4[3]), cvtpk(d8[0], d8[1]), cvtpk(d8[2], d8[3]));
      bf16x8 w1 = cat4(cvtpk(d12[0], d12[1]), cvtpk(d12[2], d12[3]), cvtpk(d16[0], d16[1]), cvtpk(d16[2], d16[3]));
#pragma unroll
      for (int dl = 2; dl <= 4; ++dl)
        if (i + dl < 8) { acc[i + dl] = MFMA16(w0, KM[dl - 2][0], acc[i + dl]); acc[i + dl] = MFMA16(w1, KM[dl - 2][1], acc[i + dl]); }
      if (i < 4) {
        f32x4 f8 = MFMA16(u16f, b8_3, z4), f16 = MFMA16(u24f, b8_4, z4);
        bf16x8 wf = cat4(cvtpk(f8[0], f8[1]), cvtpk(f8[2], f8[3]), cvtpk(f16[0], f16[1]), cvtpk(f16[2], f16[3]));
#pragma unroll
        for (int dl = 4; dl <= 7; ++dl)
          if (i + dl < 8) acc[i + dl] = MFMA16(wf, KF[dl - 4], acc[i + dl]);
        acc[i + 4] = MFMA16(KC, u16f, acc[i + 4]);
      }
    }
  }
  STAMP(6);
  {
    bf16x8 KA[17];
#pragma unroll
    for (int q = 0; q < 17; ++q) KA[q] = cat(ld2(kn - 64 * q), ld2(kn - 64 * q + 8));
    bf16x8 F[64];
    lptr ystg = lds + A_STG;
    lptr yw = ystg + wid * A_YS + a16 * 32 + g4 * 8;
    const int q4 = (lane >> 2) & 3, pp = lane & 3;
    lptr yr = ystg + ((g4 & 1) * 4 + q4) * A_YS + (wid * 32 + (g4 >> 1) * 16 + pp * 4) * 2;
    float* op = P.out + (size_t)(wid * 32 + (g4 >> 1) * 16 + a16) * (2 * D) + cg * 8 + (g4 & 1) * 4;
#pragma unroll
    for (int j = 0; j < 8; ++j) {
#pragma unroll
      for (int n = 8 * j; n < 8 * j + 8; ++n) F[n] = lds128(ub + 32 * (2 * n - 15));
#pragma unroll
      for (int q = 16; q >= 0; --q) {
        const int n = 8 * j + 7 - q;
        if (n >= 0) acc[j] = MFMA16(KA[q], F[n], acc[j]);
      }
      u32x2 yv = {cvtpk(acc[j][0], acc[j][1]), cvtpk(acc[j][2], acc[j][3])};
      *(LAS a_u32x2*)(yw + j * 512) = yv;
      if (j & 1) {
        BAR();
#pragma unroll
        for (int jj = j - 1; jj <= j; ++jj) {
          u32x2 t = __builtin_bit_cast(u32x2, ldstr(yr + jj * 512));
          f32x4 o = {bflo(t.x), bfhi(t.x), bflo(t.y), bfhi(t.y)};
          *(f32x4*)(op + (size_t)jj * 256 * (2 * D)) = o;
        }
      }
    }
  }
  STAMP(7);
}

constexpr int B1_STG = 32768, B1_X = 4 * B1_STG;

__device__ __forceinline__ void partB1(const Params& P, int chunk, int grp, lptr lds, u64* stamps) {
  const int tid = threadIdx.x, lane = tid & 63, wid = __builtin_amdgcn_readfirstlane(tid >> 6);
  const unsigned lds0 = (unsigned)(uintptr_t)lds;
  const int tq = wid >> 1, pq = wid & 1, r32 = lane & 31, hh = lane >> 5;
  unsigned* epw = (unsigned*)(P.ws + WS_EPOCH) + grp * 64;
  unsigned lre, lim, ldt, lre2, lim2, ldt2, ep;
  {
    unsigned off = (unsigned)((grp * 64 + pq * 32 + r32) * 2), off2 = (unsigned)((grp * 64 + lane) * 2), z = 0;
    asm volatile("global_load_ushort %0, %1, %2" : "=v"(lre) : "v"(off), "s"(P.Lre) : "memory");
    asm volatile("global_load_ushort %0, %1, %2" : "=v"(lim) : "v"(off), "s"(P.Lim) : "memory");
    asm volatile("global_load_ushort %0, %1, %2" : "=v"(ldt) : "v"(off), "s"(P.ldt_b) : "memory");
    asm volatile("global_load_ushort %0, %1, %2" : "=v"(lre2) : "v"(off2), "s"(P.Lre) : "memory");
    asm volatile("global_load_ushort %0, %1, %2" : "=v"(lim2) : "v"(off2), "s"(P.Lim) : "memory");
    asm volatile("global_load_ushort %0, %1, %2" : "=v"(ldt2) : "v"(off2), "s"(P.ldt_b) : "memory");
    asm volatile("global_load_dword %0, %1, %2 sc1" : "=v"(ep) : "v"(z), "s"(epw) : "memory");
  }
  const char* xs = (const char*)P.x + (size_t)chunk * 128 * ROWB;
  const char* wr = (const char*)P.Bre + (size_t)grp * 64 * ROWB;
  const char* wi = (const char*)P.Bim + (size_t)grp * 64 * ROWB;
  const unsigned voff = (unsigned)((8 * wid + (lane >> 3)) * ROWB + (((lane & 7) ^ (4 * (wid & 1) + (lane >> 4))) << 4));
  auto issue = [&](int s) {
    const unsigned dst = lds0 + (s % 5) * B1_STG + wid * 1024;
    glds16(xs + s * 128, voff, dst);
    glds16(wr + s * 128, voff, dst + 16384);
    glds16(xs + 64 * ROWB + s * 128, voff, dst + 8192);
    glds16(wi + s * 128, voff, dst + 24576);
  };
#pragma unroll
  for (int s = 0; s < 5; ++s) issue(s);
  asm volatile("s_waitcnt vmcnt(20)" : "+v"(lre), "+v"(lim), "+v"(ldt), "+v"(lre2), "+v"(lim2), "+v"(ldt2), "+v"(ep)::"memory");
  const unsigned tag = ep + 1u;
  f32x2 lam, coef, lam2, lam16, lam16b, pw0, pw1, lrun;
  {
    float lr = bflo(lre), li = bflo(lim), dt = rnd_bf16(expf(bflo(ldt)));
    float m = expf(dt * lr), sn, cs;
    sincosf(dt * li, &sn, &cs);
    lam = f32x2{m * cs, m * sn};
    float inv = 1.f / (lr * lr + li * li);
    coef = cmul2(f32x2{lam.x - 1.f, lam.y}, f32x2{lr * inv, -li * inv});
    lam2 = cmul2(lam, lam);
    f32x2 t = cmul2(lam2, lam2); t = cmul2(t, t); lam16 = cmul2(t, t);
    f32x2 l32 = cmul2(lam16, lam16);
    lrun = hh ? lam16 : f32x2{1.f, 0.f};
    for (int i = 0; i < tq; ++i) lrun = cmul2(lrun, l32);
    float lr2 = bflo(lre2), li2 = bflo(lim2), dt2 = rnd_bf16(expf(bflo(ldt2)));
    float m2 = expf(dt2 * lr2), sn2, cs2;
    sincosf(dt2 * li2, &sn2, &cs2);
    f32x2 v = f32x2{m2 * cs2, m2 * sn2};
#pragma unroll
    for (int i = 0; i < 4; ++i) v = cmul2(v, v);
    lam16b = v;
#pragma unroll
    for (int i = 0; i < 3; ++i) v = cmul2(v, v);
    pw1 = f32x2{1.f, 0.f};
    for (int i = 0; i < chunk - 2 - 2 * wid; ++i) pw1 = cmul2(pw1, v);
    pw0 = (chunk - 1 == 2 * wid) ? f32x2{1.f, 0.f} : cmul2(pw1, v);
  }
  const int xrow = tq * 32 + 16 * ((r32 >> 2) & 1) + (r32 & 3) + 4 * (r32 >> 3);
  const int wrow = pq * 32 + r32;
  const unsigned xa = xrow * 128 + ((hh ^ ((xrow >> 1) & 7)) << 4);
  const unsigned wa = 16384 + wrow * 128 + ((hh ^ ((wrow >> 1) & 7)) << 4);
  f32x16 are = {}, aim = {};
  STAMP(1);
#pragma unroll
  for (int s = 0; s < 9; ++s) {
    if (s == 0) waitv_bar<16>(); else if (s <= 5) waitv_bar<12>(); else if (s == 6) waitv_bar<8>(); else if (s == 7) waitv_bar<4>(); else waitv_bar<0>();
    if (s >= 1 && s + 4 < 9) issue(s + 4);
    lptr st = lds + (s % 5) * B1_STG;
#pragma unroll
    for (int ks = 0; ks < 4; ++ks) {
      bf16x8 a = lds128(st + (xa ^ (ks << 5))), b0 = lds128(st + (wa ^ (ks << 5))), b1 = lds128(st + (wa ^ (ks << 5)) + 8192);
      are = MFMA32(a, b0, are);
      aim = MFMA32(a, b1, aim);
    }
  }
  STAMP(2);
  const int run = 2 * tq + hh, pl = pq * 32 + r32;
  lptr xs_ = lds + B1_X;
  {
    f32x2 sr = {are[0], are[1]}, si = {aim[0], aim[1]};
    const f32x2 l2r = lam2.xx, l2i = lam2.yy;
#pragma unroll
    for (int k = 1; k < 8; ++k) {
      f32x2 re = {are[2 * k], are[2 * k + 1]}, im = {aim[2 * k], aim[2 * k + 1]};
      f32x2 nr = __builtin_elementwise_fma(l2r, sr, re), ni = __builtin_elementwise_fma(l2r, si, im);
      nr = __builtin_elementwise_fma(-l2i, si, nr); ni = __builtin_elementwise_fma(l2i, sr, ni);
      sr = nr; si = ni;
    }
    f32x2 s = cfma(f32x2{sr.x, si.x}, mkcm(lam), f32x2{sr.y, si.y});
    s = cmul2(s, coef);
    *(LAS a_f32x2*)(xs_ + (run * 64 + pl) * 8) = s;
  }
  BAR();
  u32x4* gran = (u32x4*)(P.ws + WS_GRAN) + grp * 16 * 64;
  const __amdgpu_buffer_rsrc_t gr = __builtin_amdgcn_make_buffer_rsrc(gran, 0, 16 * 64 * 16, 0x00020000);
  if (wid == 7 && chunk < 15) {
    CM m = mkcm(lam16b);
    f32x2 t = *(LAS a_f32x2*)(xs_ + lane * 8);
#pragma unroll
    for (int r = 1; r < 8; ++r) t = cfma(t, m, *(LAS a_f32x2*)(xs_ + (r * 64 + lane) * 8));
    u32x4 g = {asu(t.x), tag, asu(t.y), tag};
    __builtin_amdgcn_raw_buffer_store_b128(g, gr, (chunk * 64 + lane) * 16, 0, 16);
  }
  STAMP(3);
  f32x2 lpre = {0.f, 0.f};
  {
    CM m = mkcm(lam16);
    for (int r = 0; r < 2 * tq; ++r) lpre = cfma(lpre, m, *(LAS a_f32x2*)(xs_ + (r * 64 + pl) * 8));
    f32x2 e = cfma(lpre, m, *(LAS a_f32x2*)(xs_ + (2 * tq * 64 + pl) * 8));
    if (hh) lpre = e;
  }
  f32x2 pre[8], pim[8];
  {
    const f32x2 cr = coef.xx, ci = coef.yy;
#pragma unroll
    for (int k = 0; k < 8; ++k) {
      f32x2 re = {are[2 * k], are[2 * k + 1]}, im = {aim[2 * k], aim[2 * k + 1]};
      f32x2 nr = __builtin_elementwise_fma(-ci, im, cr * re), ni = __builtin_elementwise_fma(ci, re, cr * im);
      are[2 * k] = nr.x; are[2 * k + 1] = nr.y; aim[2 * k] = ni.x; aim[2 * k + 1] = ni.y;
    }
#pragma unroll
    for (int t = 1; t < 16; ++t) {
      float nr = fmaf(lam.x, are[t - 1], are[t]), ni = fmaf(lam.x, aim[t - 1], aim[t]);
      are[t] = fmaf(-lam.y, aim[t - 1], nr); aim[t] = fmaf(lam.y, are[t - 1], ni);
    }
    pre[0] = f32x2{lam.x, lam2.x}; pim[0] = f32x2{lam.y, lam2.y};
    const f32x2 l2r = lam2.xx, l2i = lam2.yy;
#pragma unroll
    for (int k = 1; k < 8; ++k) {
      pre[k] = __builtin_elementwise_fma(-l2i, pim[k - 1], l2r * pre[k - 1]);
      pim[k] = __builtin_elementwise_fma(l2i, pre[k - 1], l2r * pim[k - 1]);
    }
  }
  {
    f32x2 part = {0.f, 0.f};
    const int c0 = 2 * wid, c1 = 2 * wid + 1;
    if (c0 < chunk) {
      const int c1c = c1 < chunk ? c1 : c0;
      u32x4 g0, g1;
      for (unsigned spin = 0; spin < 200000u; ++spin) {
        g0 = __builtin_amdgcn_raw_buffer_load_b128(gr, (c0 * 64 + lane) * 16, 0, 16);
        g1 = __builtin_amdgcn_raw_buffer_load_b128(gr, (c1c * 64 + lane) * 16, 0, 16);
        bool ok = g0.y == tag && g0.w == tag && g1.y == tag && g1.w == tag;
        if (__all(ok)) break;
        __builtin_amdgcn_s_sleep(1);
      }
      part = cmul2(f32x2{asf(g0.x), asf(g0.z)}, pw0);
      if (c1 < chunk) part = cfma(f32x2{asf(g1.x), asf(g1.z)}, mkcm(pw1), part);
    }
    *(LAS a_f32x2*)(xs_ + 4096 + (wid * 64 + lane) * 8) = part;
  }
  BAR();
  STAMP(4);
  if (chunk == 15 && tid == 0) __hip_atomic_store((GAS unsigned*)epw, tag, __ATOMIC_RELAXED, __HIP_MEMORY_SCOPE_AGENT);
  f32x2 H = {0.f, 0.f};
#pragma unroll
  for (int w = 0; w < 8; ++w) H += *(LAS a_f32x2*)(xs_ + 4096 + (w * 64 + pl) * 8);
  const f32x2 e = cfma(H, mkcm(lrun), lpre);
  unsigned hr[8], hi[8];
  {
    const f32x2 er = e.xx, ei = e.yy;
#pragma unroll
    for (int k = 0; k < 8; ++k) {
      f32x2 re = {are[2 * k], are[2 * k + 1]}, im = {aim[2 * k], aim[2 * k + 1]};
      re = __builtin_elementwise_fma(-ei, pim[k], __builtin_elementwise_fma(er, pre[k], re));
      im = __builtin_elementwise_fma(ei, pre[k], __builtin_elementwise_fma(er, pim[k], im));
      hr[k] = cvtpk(re.x, re.y); hi[k] = cvtpk(im.x, im.y);
    }
  }
  char* hb = P.ws + WS_H + (size_t)((chunk * 4 + tq) * 18 + grp * 2 + pq) * 4096;
  *(u32x4*)(hb + lane * 16) = u32x4{hr[0], hr[1], hr[2], hr[3]};
  *(u32x4*)(hb + 1024 + (lane ^ 4) * 16) = u32x4{hr[4], hr[5], hr[6], hr[7]};
  *(u32x4*)(hb + 2048 + lane * 16) = u32x4{hi[0], hi[1], hi[2], hi[3]};
  *(u32x4*)(hb + 3072 + (lane ^ 4) * 16) = u32x4{hi[4], hi[5], hi[6], hi[7]};
  STAMP(5);
}

__global__ __launch_bounds__(512) void k1(Params P) {
  extern __shared__ __attribute__((aligned(16))) char smem[];
  lptr lds = (lptr)smem;
  const int xcd = blockIdx.x & 7, loc = blockIdx.x >> 3;
  u64 stamps[10];
  STAMP(0);
  if (loc < 9) { partA(P, xcd * 9 + loc, lds, stamps); FLUSH(blockIdx.x, 1, 8); }
  else if (loc < 27) { partB1(P, 2 * xcd + (loc - 9) / 9, (loc - 9) % 9, lds, stamps); FLUSH(blockIdx.x, 2, 6); }
  else {
    const int lane = threadIdx.x & 63, wid = __builtin_amdgcn_readfirstlane(threadIdx.x >> 6);
    const unsigned lds0 = (unsigned)(uintptr_t)lds;
    for (int i = (loc - 27) * 8 + wid; i < 648; i += 40) {
      glds16((const char*)P.Cre + i * 1024, lane * 16, lds0 + wid * 2048);
      glds16((const char*)P.Cim + i * 1024, lane * 16, lds0 + wid * 2048 + 1024);
    }
    WAITV(0);
    STAMP(1);
    FLUSH(blockIdx.x, 3, 2);
  }
}

constexpr int B2_STG = 40960;
__global__ __launch_bounds__(512) void k2(Params P) {
  extern __shared__ __attribute__((aligned(16))) char smem[];
  lptr lds = (lptr)smem;
  const int xcd = blockIdx.x & 7, loc = blockIdx.x >> 3;
  u64 stamps[10];
  STAMP(0);
  const int rg = xcd * 4 + loc / 6, cgp = loc % 6;
  const int tid = threadIdx.x, lane = tid & 63, wid = __builtin_amdgcn_readfirstlane(tid >> 6);
  const unsigned lds0 = (unsigned)(uintptr_t)lds;
  const int rh = wid & 1, chf = (wid >> 1) & 1, kh = wid >> 2;
  const int a16 = lane & 15, g4 = lane >> 4;
  const int trow = rg * 64 + rh * 32 + kh * 16 + a16, dcol = cgp * 96 + chf * 48 + g4 * 4;
  u32x2 xv[3], dv[3];
  {
    unsigned xo = (unsigned)(trow * ROWB + dcol * 2), dO = (unsigned)(dcol * 2);
#pragma unroll
    for (int ct = 0; ct < 3; ++ct) {
      asm volatile("global_load_dwordx2 %0, %1, %2 offset:%3" : "=v"(xv[ct]) : "v"(xo), "s"(P.x), "i"(ct * 32) : "memory");
      asm volatile("global_load_dwordx2 %0, %1, %2 offset:%3" : "=v"(dv[ct]) : "v"(dO), "s"(P.dsk_b), "i"(ct * 32) : "memory");
    }
  }
  const char* hs = P.ws + WS_H + (size_t)(rg * 2) * 18 * 4096;
  const unsigned hoff = (unsigned)(wid * 1024 + lane * 16);
  const unsigned coff = (unsigned)((lane >> 3) * ROWB + (((lane & 7) ^ (4 * (wid & 1) + (lane >> 4))) << 4));
  const char* c0 = (const char*)P.Cre + (size_t)(cgp * 96 + 8 * wid) * ROWB;
  const char* c1 = wid < 4 ? (const char*)P.Cre + (size_t)(cgp * 96 + 64 + 8 * wid) * ROWB : (const char*)P.Cim + (size_t)(cgp * 96 + 8 * wid - 32) * ROWB;
  const char* c2 = (const char*)P.Cim + (size_t)(cgp * 96 + 32 + 8 * wid) * ROWB;
  auto issue = [&](int s) {
    const unsigned dst = lds0 + (s & 3) * B2_STG + wid * 1024;
    glds16(hs + s * 8192, hoff, dst);
    glds16(hs + 18 * 4096 + s * 8192, hoff, dst + 8192);
    glds16(c0 + s * 128, coff, dst + 16384);
    glds16(c1 + s * 128, coff, dst + 24576);
    glds16(c2 + s * 128, coff, dst + 32768);
  };
#pragma unroll
  for (int s = 0; s < 4; ++s) issue(s);
  const int q4 = (lane >> 2) & 3, pp = lane & 3, xq = pp >> 1;
  const unsigned hb0 = rh * 8192 + (2 * kh + xq) * 1024 + (8 * g4 + q4 + 4 * xq) * 16 + (pp & 1) * 8;
  const unsigned hb1 = rh * 8192 + (2 * kh + xq) * 1024 + (8 * g4 + q4 + 4 * (1 - xq)) * 16 + (pp & 1) * 8;
  const unsigned ca = 16384 + (kh * 96 + chf * 48 + a16) * 128 + ((g4 ^ (a16 >> 1)) << 4);
  f32x4 acc[2][3];
#pragma unroll
  for (int n = 0; n < 2; ++n)
#pragma unroll
    for (int ct = 0; ct < 3; ++ct) acc[n][ct] = f32x4{0.f, 0.f, 0.f, 0.f};
#pragma unroll
  for (int s = 0; s < 9; ++s) {
    if (s == 0) waitv_bar<15>(); else if (s <= 6) waitv_bar<10>(); else if (s == 7) waitv_bar<5>(); else waitv_bar<0>();
    if (s == 0) STAMP(1);
    if (s >= 1 && s + 3 < 9) issue(s + 3);
    lptr st = lds + (s & 3) * B2_STG;
#pragma unroll
    for (int pb = 0; pb < 2; ++pb) {
      bf16x8 hf[2], cf[3];
#pragma unroll
      for (int n = 0; n < 2; ++n)
        hf[n] = cat(__builtin_bit_cast(u32x2, ldstr(st + hb0 + pb * 4096 + n * 512)), __builtin_bit_cast(u32x2, ldstr(st + hb1 + pb * 4096 + n * 512)));
#pragma unroll
      for (int ct = 0; ct < 3; ++ct) cf[ct] = lds128(st + (ca ^ (pb << 6)) + ct * 2048);
#pragma unroll
      for (int n = 0; n < 2; ++n)
#pragma unroll
        for (int ct = 0; ct < 3; ++ct) acc[n][ct] = MFMA16(cf[ct], hf[n], acc[n][ct]);
    }
  }
  STAMP(2);
  lptr ex = lds + B2_STG;
#pragma unroll
  for (int ct = 0; ct < 3; ++ct) *(LAS a_f32x4*)(ex + (wid * 3 + ct) * 1024 + lane * 16) = kh ? acc[0][ct] : acc[1][ct];
  BAR();
  asm volatile("" : "+v"(xv[0]), "+v"(xv[1]), "+v"(xv[2]), "+v"(dv[0]), "+v"(dv[1]), "+v"(dv[2]));
  float* op = P.out + (size_t)trow * (2 * D) + D + dcol;
#pragma unroll
  for (int ct = 0; ct < 3; ++ct) {
    f32x4 o = *(LAS a_f32x4*)(ex + ((wid ^ 4) * 3 + ct) * 1024 + lane * 16);
    f32x4 y = kh ? (o - acc[1][ct]) : (acc[0][ct] - o);
    unsigned s0 = cvtpk(bflo(xv[ct].x) * bflo(dv[ct].x), bfhi(xv[ct].x) * bfhi(dv[ct].x));
    unsigned s1 = cvtpk(bflo(xv[ct].y) * bflo(dv[ct].y), bfhi(xv[ct].y) * bfhi(dv[ct].y));
    f32x4 r = {fmaf(2.f, y[0], bflo(s0)), fmaf(2.f, y[1], bfhi(s0)), fmaf(2.f, y[2], bflo(s1)), fmaf(2.f, y[3], bfhi(s1))};
    *(f32x4*)(op + ct * 16) = r;
  }
  STAMP(3);
  FLUSH(256 + blockIdx.x, 4, 4);
}

extern "C" void kernel_launch(void* const* d_in, const int* in_sizes, int n_in, void* d_out, int out_size, void* d_ws,
                              size_t ws_size, hipStream_t stream) {
  Params P;
  P.u = (const u16*)d_in[0]; P.Are = (const u16*)d_in[1]; P.Aim = (const u16*)d_in[2]; P.Bre_a = (const u16*)d_in[3];
  P.Bim_a = (const u16*)d_in[4]; P.Cre_a = (const u16*)d_in[5]; P.Cim_a = (const u16*)d_in[6]; P.ldt_a = (const u16*)d_in[7];
  P.dsk_a = (const u16*)d_in[8]; P.x = (const u16*)d_in[9]; P.Lre = (const u16*)d_in[10]; P.Lim = (const u16*)d_in[11];
  P.Bre = (const u16*)d_in[12]; P.Bim = (const u16*)d_in[13]; P.Cre = (const u16*)d_in[14]; P.Cim = (const u16*)d_in[15];
  P.ldt_b = (const u16*)d_in[16]; P.dsk_b = (const u16*)d_in[17];
  P.out = (float*)d_out; P.ws = (char*)d_ws;
  (void)hipFuncSetAttribute((const void*)k1, hipFuncAttributeMaxDynamicSharedMemorySize, 163840);
  (void)hipFuncSetAttribute((const void*)k2, hipFuncAttributeMaxDynamicSharedMemorySize, 163840);
  k1<<<256, 512, 163840, stream>>>(P);
  k2<<<192, 512, 163840, stream>>>(P);
}
```

```cpp
#include <hip/hip_runtime.h>
#include <stdint.h>

#define LAS __attribute__((address_space(3)))
#define GAS __attribute__((address_space(1)))
typedef short bf16x8 __attribute__((ext_vector_type(8)));
typedef short s16x4 __attribute__((ext_vector_type(4)));
typedef float f32x2 __attribute__((ext_vector_type(2)));
typedef float f32x4 __attribute__((ext_vector_type(4)));
typedef float f32x16 __attribute__((ext_vector_type(16)));
typedef unsigned u32x2 __attribute__((ext_vector_type(2)));
typedef unsigned u32x4 __attribute__((ext_vector_type(4)));
typedef __bf16 bf2 __attribute__((ext_vector_type(2)));
typedef unsigned long long u64;
typedef unsigned short u16;
typedef LAS char* lptr;
#define MA __attribute__((may_alias))
typedef bf16x8 MA a_bf16x8; typedef s16x4 MA a_s16x4; typedef f32x2 MA a_f32x2; typedef f32x4 MA a_f32x4;
typedef u32x2 MA a_u32x2; typedef u32x4 MA a_u32x4; typedef unsigned MA a_u32; typedef u16 MA a_u16;

#define MFMA16(a, b, c) __builtin_amdgcn_mfma_f32_16x16x32_bf16(a, b, c, 0, 0, 0)
#define MFMA32(a, b, c) __builtin_amdgcn_mfma_f32_32x32x16_bf16(a, b, c, 0, 0, 0)

#define SB() __builtin_amdgcn_sched_barrier(0)
#define STAMP(k) do { } while (0)
#define FLUSH(wg, r, n) do { } while (0)

constexpr int L = 2048, D = 576, ROWB = D * 2;
constexpr size_t WS_H = 0, WS_GRAN = 8u << 20, WS_EPOCH = 9u << 20, WS_FLAG = 10u << 20, WS_STAMP = 200u << 20;

struct Params {
  const u16* u; const u16* Are; const u16* Aim; const u16* Bre_a; const u16* Bim_a; const u16* Cre_a; const u16* Cim_a;
  const u16* ldt_a; const u16* dsk_a;
  const u16* x; const u16* Lre; const u16* Lim; const u16* Bre; const u16* Bim; const u16* Cre; const u16* Cim;
  const u16* ldt_b; const u16* dsk_b;
  float* out; char* ws;
};

__device__ __forceinline__ unsigned cvtpk(float a, float b) {
  f32x2 v = {a, b};
  return __builtin_bit_cast(unsigned, __builtin_convertvector(v, bf2));
}
__device__ __forceinline__ float asf(unsigned v) { return __builtin_bit_cast(float, v); }
__device__ __forceinline__ unsigned asu(float v) { return __builtin_bit_cast(unsigned, v); }
__device__ __forceinline__ float bflo(unsigned v) { return __builtin_bit_cast(float, v << 16); }
__device__ __forceinline__ float bfhi(unsigned v) { return __builtin_bit_cast(float, v & 0xffff0000u); }
__device__ __forceinline__ float rnd_bf16(float v) { return bflo(cvtpk(v, v)); }
struct C1 { float r, i; };
__device__ __forceinline__ float opq(float v) { asm("" : "+v"(v)); return v; }
__device__ __forceinline__ C1 cmul(C1 a, C1 b) { return C1{opq(fmaf(a.r, b.r, -(a.i * b.i))), opq(fmaf(a.r, b.i, a.i * b.r))}; }
__device__ __forceinline__ C1 cfma(C1 a, C1 b, C1 c) { return C1{opq(fmaf(-a.i, b.i, fmaf(a.r, b.r, c.r))), opq(fmaf(a.i, b.r, fmaf(a.r, b.i, c.i)))}; }
struct C2 { f32x2 r, i; };
#define PFMA __builtin_elementwise_fma
__device__ __forceinline__ C2 cmul(const C2& a, const C2& e) { C2 o; f32x2 t0 = e.r * a.r, t1 = e.r * a.i; o.r = PFMA(-e.i, a.i, t0); o.i = PFMA(e.i, a.r, t1); return o; }
__device__ __forceinline__ f32x2 dup(float v) { f32x2 d = {v, v}; asm("" : "+v"(d)); return d; }
__device__ __forceinline__ C1 cexp1(float l2, float rev) {
  float m = __builtin_amdgcn_exp2f(l2), th = __builtin_amdgcn_fractf(rev);
  return C1{m * __builtin_amdgcn_cosf(th), m * __builtin_amdgcn_sinf(th)};
}
__device__ __forceinline__ C2 cexp2(f32x2 l2, f32x2 rev) {
  float x0 = l2.x, x1 = l2.y, t0 = __builtin_amdgcn_fractf(rev.x), t1 = __builtin_amdgcn_fractf(rev.y);
  f32x2 m = {__builtin_amdgcn_exp2f(x0), __builtin_amdgcn_exp2f(x1)};
  f32x2 c = {__builtin_amdgcn_cosf(t0), __builtin_amdgcn_cosf(t1)}, s = {__builtin_amdgcn_sinf(t0), __builtin_amdgcn_sinf(t1)};
  return C2{m * c, m * s};
}
__device__ __forceinline__ float exp_acc(float x) {
  float hi = x * 1.4426950216293335f;
  float lo = fmaf(x, 1.925963033500011e-8f, fmaf(x, 1.4426950216293335f, -hi));
  float m = __builtin_amdgcn_exp2f(hi);
  return fmaf(m, lo * 0.6931471805599453f, m);
}
constexpr float LOG2E = 1.4426950408889634f, INV2PI = 0.15915494309189535f;
__device__ __forceinline__ void glds16(const void* sbase, unsigned voff, unsigned dst) {
  asm volatile("s_mov_b32 m0, %2\n\ts_nop 0\n\tglobal_load_lds_dwordx4 %0, %1" ::"v"(voff), "s"(sbase), "s"(dst) : "memory");
}
template <int DST, int IMM> __device__ __forceinline__ void glds16o(const void* sbase, unsigned voff, unsigned dbase) {
  asm volatile("s_add_i32 m0, %2, %3\n\ts_nop 0\n\tglobal_load_lds_dwordx4 %0, %1 offset:%4" ::"v"(voff), "s"(sbase), "s"(dbase), "i"(DST - IMM), "i"(IMM) : "memory", "scc");
}
#define WAITV(n) asm volatile("s_waitcnt vmcnt(" #n ")" ::: "memory")
#define BAR() asm volatile("s_waitcnt lgkmcnt(0)\n\ts_barrier" ::: "memory")
template <int N> __device__ __forceinline__ void waitv_bar() { asm volatile("s_waitcnt vmcnt(%0) lgkmcnt(0)\n\ts_barrier" ::"i"(N) : "memory"); }
__device__ __forceinline__ bf16x8 lds128(lptr p) { return *(LAS a_bf16x8*)p; }
__device__ __forceinline__ s16x4 ldstr(lptr p) { return __builtin_amdgcn_ds_read_tr16_b64_v4i16((LAS s16x4*)p); }
__device__ __forceinline__ bf16x8 cat(u32x2 a, u32x2 b) { u32x4 v = {a.x, a.y, b.x, b.y}; return __builtin_bit_cast(bf16x8, v); }
__device__ __forceinline__ bf16x8 cat4(unsigned a, unsigned b, unsigned c, unsigned d) { u32x4 v = {a, b, c, d}; return __builtin_bit_cast(bf16x8, v); }

#define UD(x) x
constexpr int A_REG = 16384;
constexpr int A_STG = 8 * A_REG;
constexpr int A_R1 = 2112, A_UT = 4352;
constexpr int SQ_N = 0, SQ_M = 560, SQ_F = 560 + 4 * 64;
typedef const __attribute__((address_space(4))) unsigned* cptr;

__device__ __forceinline__ u32x2 ld2(lptr p) { u32x2 r; r.x = *(LAS a_u32*)p; r.y = *(LAS a_u32*)(p + 4); return r; }

__device__ __forceinline__ bf16x8 band(int lane, int ctr, int h) {
  int base = (lane & 15) + ctr - 8 * (lane >> 4);
  unsigned w[4];
#pragma unroll
  for (int j = 0; j < 4; ++j) {
    int d0 = base - 2 * j, d1 = d0 - 1;
    unsigned lo = ((unsigned)(d0 + h) <= (unsigned)(2 * h)) ? 0x3f80u : 0u;
    unsigned hi = ((unsigned)(d1 + h) <= (unsigned)(2 * h)) ? 0x3f800000u : 0u;
    w[j] = lo | hi;
  }
  return cat4(w[0], w[1], w[2], w[3]);
}

__device__ __forceinline__ void partA(const Params& P, int cg, lptr lds, u64* stamps) {
  const int tid = threadIdx.x, lane = tid & 63, wid = __builtin_amdgcn_readfirstlane(tid >> 6);
  const int ch = cg * 8 + wid;
  const unsigned lds0 = (unsigned)(uintptr_t)lds;
  unsigned pa[6];
  {
    unsigned off = (unsigned)(ch * 256 + lane * 4);
    asm volatile("global_load_dword %0, %1, %2" : "=v"(pa[0]) : "v"(off), "s"(P.Are) : "memory");
    asm volatile("global_load_dword %0, %1, %2" : "=v"(pa[1]) : "v"(off), "s"(P.Aim) : "memory");
    asm volatile("global_load_dword %0, %1, %2" : "=v"(pa[2]) : "v"(off), "s"(P.Bre_a) : "memory");
    asm volatile("global_load_dword %0, %1, %2" : "=v"(pa[3]) : "v"(off), "s"(P.Bim_a) : "memory");
    asm volatile("global_load_dword %0, %1, %2" : "=v"(pa[4]) : "v"(off), "s"(P.Cre_a) : "memory");
    asm volatile("global_load_dword %0, %1, %2" : "=v"(pa[5]) : "v"(off), "s"(P.Cim_a) : "memory");
  }
  const unsigned ldw = ((cptr)P.ldt_a)[ch >> 1], dsw = ((cptr)P.dsk_a)[ch >> 1];
  lptr reg = lds + wid * A_REG;
  const int a16 = lane & 15, g4 = lane >> 4, r32 = lane & 31, hh = lane >> 5;
  bf16x8 b12_1 = band(lane, 12, 1), b8_2 = band(lane, 8, 2), b4_1 = band(lane, 4, 1), b8_3 = band(lane, 8, 3), b8_4 = band(lane, 8, 4);
  lptr wr[16], rd[8];
#pragma unroll
  for (int r = 0; r < 16; ++r) wr[r] = reg + ((lane * 8) ^ (r << 4));
#pragma unroll
  for (int k = 0; k < 8; ++k) rd[k] = reg + r32 * 512 + (((hh ^ (r32 & 15)) << 4) ^ (k << 5));
  const float dt = rnd_bf16(exp_acc((ch & 1) ? bfhi(ldw) : bflo(ldw)));
  const float dskip = (ch & 1) ? bfhi(dsw) : bflo(dsw);
  f32x2 dt2 = dup(dt), dl2 = dup(LOG2E), dr2 = dup(INV2PI), c8 = dup(8.f), c256 = dup(256.f);
  const char* ucol = (const char*)P.u + cg * 16;
  const unsigned uoff = (unsigned)((wid * 256 + lane) * ROWB), udst = lds0 + A_STG + wid * 4096;
  asm volatile("" : "+v"(b12_1), "+v"(b8_2), "+v"(b4_1), "+v"(b8_3), "+v"(b8_4), "+v"(dt2));
  asm volatile("" : "+v"(wr[0]), "+v"(wr[1]), "+v"(wr[2]), "+v"(wr[3]), "+v"(wr[4]), "+v"(wr[5]), "+v"(wr[6]), "+v"(wr[7]),
               "+v"(wr[8]), "+v"(wr[9]), "+v"(wr[10]), "+v"(wr[11]), "+v"(wr[12]), "+v"(wr[13]), "+v"(wr[14]), "+v"(wr[15]));
  asm volatile("" : "+v"(rd[0]), "+v"(rd[1]), "+v"(rd[2]), "+v"(rd[3]), "+v"(rd[4]), "+v"(rd[5]), "+v"(rd[6]), "+v"(rd[7]));
  asm volatile("s_waitcnt vmcnt(0)" : "+v"(pa[0]), "+v"(pa[1]), "+v"(pa[2]), "+v"(pa[3]), "+v"(pa[4]), "+v"(pa[5])::"memory");
  STAMP(1);
  f32x16 kacc = {};
  {
    auto un = [](unsigned v) { return f32x2{bflo(v), bfhi(v)}; };
    auto put = [&](int row, const C2& v) { *(LAS a_u32x2*)(wr[row & 15] + row * 512) = u32x2{cvtpk(v.r.x, v.r.y), cvtpk(v.i.x, v.i.y)}; };
    const f32x2 l2 = (dt2 * un(pa[0])) * dl2, rv = (dt2 * un(pa[1])) * dr2;
    const C2 e8 = cexp2(l2 * c8, rv * c8);
    const C2 W = cmul(C2{un(pa[4]), un(pa[5])}, C2{dt2 * un(pa[2]), dt2 * un(pa[3])});
    C2 x1 = cexp2(l2, rv), x256 = cexp2(l2 * c256, rv * c256);
    x1.i = -x1.i; x256.i = -x256.i;
    const C2 x8{e8.r, -e8.i};
    UD(glds16(ucol, uoff, udst));
    C2 p{W.r, -W.i};
    const C2 q256 = cmul(p, x256);
    C2 q260;
    put(0, p); put(8, q256);
#pragma unroll
    for (int a = 1; a < 8; ++a) {
      p = cmul(p, x1); put(a, p);
      if ((a & 1) == 0) { C2 t = cmul(p, x256); put(8 + a / 2, t); if (a == 4) q260 = t; }
    }
    UD(glds16(ucol, uoff + 64 * ROWB, udst + 1024));
    const C2 q264 = cmul(q256, x8); put(16, q264);
    const C2 q516 = cmul(q260, x256), q520 = cmul(q264, x256); put(12, q516); put(13, q520);
    const C2 q772 = cmul(q516, x256);
    C2 q = cmul(q520, x256); put(14, q772); put(15, q);
#pragma unroll
    for (int k = 0; k < 4; ++k) { q = cmul(q, x256); put(20 + k, q); }
    SB();
    bf16x8 fa[16];
#pragma unroll
    for (int ks = 0; ks < 16; ++ks) fa[ks] = lds128(rd[ks & 7] + (ks >> 3) * 256);
    SB();
    UD(glds16(ucol, uoff + 128 * ROWB, udst + 2048));
    *(LAS a_u32x2*)(wr[0]) = u32x2{0x3f803f80u, 0u};
    C2 g = e8;
    put(1, g);
#pragma unroll
    for (int c = 2; c < 32; ++c) {
      g = cmul(g, e8); put(c, g);
      if (c == 16) UD(glds16(ucol, uoff + 192 * ROWB, udst + 3072));
    }
    SB();
    STAMP(2);
    bf16x8 fb[16];
#pragma unroll
    for (int ks = 0; ks < 16; ++ks) fb[ks] = lds128(rd[ks & 7] + (ks >> 3) * 256);
    SB();
    const u32x4 z = {0u, 0u, 0u, 0u};
    *(LAS a_u32x4*)(reg + lane * 16) = z;
    *(LAS a_u32x4*)(reg + 1024 + lane * 16) = z;
    if (lane < 32) *(LAS a_u32x4*)(reg + A_UT + wid * 16 + lane * 16) = z;
    SB();
#pragma unroll
    for (int ks = 0; ks < 16; ++ks) kacc = MFMA32(fa[ks], fb[ks], kacc);
  }
  STAMP(3);
  float k1024;
  {
    const int c = r32;
    float k0 = kacc[0];
    if (lane == 0) k0 += dskip;
    u32x2 nw = {cvtpk(kacc[3], kacc[2]), cvtpk(kacc[1], k0)};
    *(LAS a_u32x2*)(reg + (SQ_N + 540 - 8 * c - 4 * hh) * 2) = nw;
    if (hh == 0) {
      u32x4 mw = {cvtpk(kacc[8], kacc[7]), cvtpk(kacc[6], kacc[6]), cvtpk(kacc[6], kacc[5]), cvtpk(kacc[4], kacc[4])};
      *(LAS a_u32x4*)(reg + (SQ_N + 280 - 8 * c) * 2) = mw;
      if (c == 31) {
        unsigned v = cvtpk(kacc[8], kacc[8]);
        *(LAS a_u32*)(reg + (SQ_N + 30) * 2) = v;
        *(LAS a_u16*)(reg + (SQ_N + 29) * 2) = (u16)v;
      }
    } else {
      const int odd = c & 1, cm = c >> 1;
      lptr m0 = reg + (SQ_M + (2 * odd) * 64 + 79 - 32 - cm) * 2;
      unsigned a = cvtpk(kacc[4], kacc[5]), b = cvtpk(kacc[6], kacc[7]);
      *(LAS a_u16*)(m0) = (u16)a; *(LAS a_u16*)(m0 + 128) = (u16)(a >> 16);
      *(LAS a_u16*)(m0 - 32) = (u16)b; *(LAS a_u16*)(m0 + 128 - 32) = (u16)(b >> 16);
      lptr f0 = reg + (SQ_F + odd * 80 + 127 - 64 - cm) * 2;
      unsigned e = cvtpk(kacc[8], kacc[9]), f = cvtpk(kacc[10], kacc[11]);
      *(LAS a_u16*)(f0) = (u16)e; *(LAS a_u16*)(f0 - 32) = (u16)(e >> 16);
      *(LAS a_u16*)(f0 - 64) = (u16)f; *(LAS a_u16*)(f0 - 96) = (u16)(f >> 16);
    }
    k1024 = asf(__builtin_amdgcn_readlane(asu(kacc[7]), 63));
    u32x4 v[2]; unsigned nx[2];
#pragma unroll
    for (int it = 0; it < 2; ++it) { lptr s = reg + it * 1024 + lane * 16; v[it] = *(LAS a_u32x4*)s; nx[it] = *(LAS a_u32*)(s + 16); }
#pragma unroll
    for (int it = 0; it < 2; ++it) {
      u32x4 o = {__builtin_amdgcn_alignbit(v[it].y, v[it].x, 16), __builtin_amdgcn_alignbit(v[it].z, v[it].y, 16),
                 __builtin_amdgcn_alignbit(v[it].w, v[it].z, 16), __builtin_amdgcn_alignbit(nx[it], v[it].w, 16)};
      *(LAS a_u32x4*)(reg + it * 1024 + lane * 16 + A_R1) = o;
    }
  }
  bf16x8 KA[17], KM[3][2], KF[4], KC;
  {
    lptr kn = reg + ((a16 & 1) ? (527 - a16 + 8 * g4) * 2 : A_R1 + (526 - a16 + 8 * g4) * 2);
    lptr kf = reg + ((a16 & 1) ? (79 - a16 + 4 * g4) * 2 : A_R1 + (78 - a16 + 4 * g4) * 2);
#pragma unroll
    for (int dl = 2; dl <= 4; ++dl)
#pragma unroll
      for (int pr = 0; pr < 2; ++pr)
        KM[dl - 2][pr] = cat(ld2(kf + (SQ_M + (2 * pr) * 64) * 2 - 32 * dl), ld2(kf + (SQ_M + (2 * pr + 1) * 64) * 2 - 32 * dl));
#pragma unroll
    for (int dl = 4; dl <= 7; ++dl) KF[dl - 4] = cat(ld2(kf + (SQ_F + 48) * 2 - 32 * dl), ld2(kf + (SQ_F + 80 + 48) * 2 - 32 * dl));
#pragma unroll
    for (int q = 0; q < 17; ++q) KA[q] = cat(ld2(kn - 64 * q), ld2(kn - 64 * q + 8));
    unsigned kb = cvtpk(k1024, k1024), w[4];
    int j0 = a16 + 12 - 8 * g4;
#pragma unroll
    for (int j = 0; j < 4; ++j)
      w[j] = (((unsigned)(2 * j - j0) <= 1u) ? (kb & 0xffffu) : 0u) | (((unsigned)(2 * j + 1 - j0) <= 1u) ? (kb & 0xffff0000u) : 0u);
    KC = cat4(w[0], w[1], w[2], w[3]);
  }
  const int q4 = (lane >> 2) & 3, pp = lane & 3;
  lptr tsrc = lds + A_STG + (wid * 256 + g4 * 8 + (pp >> 1) * 4 + q4) * 16 + (pp & 1) * 8;
  lptr tdst = lds + (a16 & 7) * (A_REG + 16) + A_UT + 512 + (wid * 256 + g4 * 8 + (a16 >> 3) * 4) * 2;
  asm volatile("" : "+v"(tsrc), "+v"(tdst), "+v"(KC));
  STAMP(4);
  SB();
  waitv_bar<0>();
  STAMP(5);
  {
    s16x4 t[8];
#pragma unroll
    for (int k = 0; k < 8; ++k) t[k] = ldstr(tsrc + k * 512);
#pragma unroll
    for (int k = 0; k < 8; ++k) *(LAS a_s16x4*)(tdst + k * 64) = t[k];
  }
  SB();
  BAR();
  STAMP(6);
  lptr ub = reg + A_UT + wid * 16 + 512 + a16 * 32 + g4 * 16;
  f32x4 acc[8];
  const f32x4 z4 = {0.f, 0.f, 0.f, 0.f};
#pragma unroll
  for (int j = 0; j < 8; ++j) acc[j] = z4;
  constexpr int LA = 4;
  bf16x8 F[64];
  {
    bf16x8 ua[6], uc[6];
#pragma unroll
    for (int i = 0; i < 6; ++i) { ua[i] = lds128(ub + 32 * (16 * i - 1)); uc[i] = lds128(ub + 32 * (16 * i - 1) - 16); }
#pragma unroll
    for (int n = 0; n < LA; ++n) F[n] = lds128(ub + 32 * (2 * n - 15));
    f32x4 d[2][6];
    auto box = [&](int i) {
      d[i & 1][0] = MFMA16(ua[i], b12_1, z4); d[i & 1][1] = MFMA16(ua[i], b8_2, z4);
      d[i & 1][2] = MFMA16(ua[i], b4_1, z4); d[i & 1][3] = MFMA16(uc[i], b8_2, z4);
      if (i < 4) { d[i & 1][4] = MFMA16(ua[i], b8_3, z4); d[i & 1][5] = MFMA16(uc[i], b8_4, z4); acc[i + 4] = MFMA16(KC, ua[i], acc[i + 4]); }
    };
    auto pk = [](const f32x4& a, const f32x4& b) { return cat4(cvtpk(a[0], a[1]), cvtpk(a[2], a[3]), cvtpk(b[0], b[1]), cvtpk(b[2], b[3])); };
    SB();
    box(0);
#pragma unroll
    for (int i = 0; i < 6; ++i) {
      SB();
      if (i < 5) box(i + 1);
      SB();
      const bf16x8 w0 = pk(d[i & 1][0], d[i & 1][1]), w1 = pk(d[i & 1][2], d[i & 1][3]);
      bf16x8 wf = w0;
      if (i < 4) wf = pk(d[i & 1][4], d[i & 1][5]);
#pragma unroll
      for (int dl = 2; dl <= 4; ++dl) if (i + dl < 8) acc[i + dl] = MFMA16(w0, KM[dl - 2][0], acc[i + dl]);
#pragma unroll
      for (int dl = 2; dl <= 4; ++dl) if (i + dl < 8) acc[i + dl] = MFMA16(w1, KM[dl - 2][1], acc[i + dl]);
      if (i < 4) {
#pragma unroll
        for (int dl = 7; dl >= 4; --dl) if (i + dl < 8) acc[i + dl] = MFMA16(wf, KF[dl - 4], acc[i + dl]);
      }
    }
  }
  STAMP(7);
  {
    lptr ystg = lds + A_STG;
    lptr yw = ystg + wid * 4096 + 32 * (a16 ^ wid) + 8 * (g4 ^ (a16 >> 2));
    const int yc = (g4 & 1) * 4 + q4;
    lptr yr = ystg + yc * 4096 + 32 * ((2 * wid + (g4 >> 1)) ^ yc) + 8 * (pp ^ (wid >> 1));
    float* op = P.out + (size_t)(wid * 32 + (g4 >> 1) * 16 + a16) * (2 * D) + cg * 8 + (g4 & 1) * 4;
    u32x2 ty[2];
    auto out2 = [&](int j) {
#pragma unroll
      for (int k = 0; k < 2; ++k) {
        f32x4 o = {bflo(ty[k].x), bfhi(ty[k].x), bflo(ty[k].y), bfhi(ty[k].y)};
        *(f32x4*)(op + (size_t)(j + k) * 256 * (2 * D)) = o;
      }
    };
#pragma unroll
    for (int n = 0; n < 64; ++n) {
      SB();
      if (n + LA < 64) F[n + LA] = lds128(ub + 32 * (2 * (n + LA) - 15));
#pragma unroll
      for (int j = 0; j < 8; ++j) {
        const int q = 8 * j + 7 - n;
        if (q >= 0 && q <= 16) acc[j] = MFMA16(KA[q], F[n], acc[j]);
      }
      if ((n & 7) == 0 && n > 0) {
        const int j = (n >> 3) - 1;
        *(LAS a_u32x2*)(yw + j * 512) = u32x2{cvtpk(acc[j][0], acc[j][1]), cvtpk(acc[j][2], acc[j][3])};
      }
      if ((n & 15) == 2 && n > 2) {
        const int j = (n >> 3) - 2;
        SB(); BAR();
        ty[0] = __builtin_bit_cast(u32x2, ldstr(yr + j * 512)); ty[1] = __builtin_bit_cast(u32x2, ldstr(yr + j * 512 + 512));
      }
      if ((n & 15) == 5 && n > 5) out2((n >> 3) - 2);
    }
    SB();
    *(LAS a_u32x2*)(yw + 7 * 512) = u32x2{cvtpk(acc[7][0], acc[7][1]), cvtpk(acc[7][2], acc[7][3])};
    BAR();
    ty[0] = __builtin_bit_cast(u32x2, ldstr(yr + 6 * 512)); ty[1] = __builtin_bit_cast(u32x2, ldstr(yr + 7 * 512));
    out2(6);
  }
  STAMP(8);
}

constexpr int B_EX = 147456;
template <int S> __device__ __forceinline__ void b_issue(const char* x0, const char* x1, const char* wr, const char* wi, unsigned voff, unsigned db) {
  constexpr int SL = (S % 5) * 32768, IM = (S % 5) * 128;
  glds16o<SL, IM>(x0, voff, db);
  glds16o<SL + 16384, IM>(wr, voff, db);
  glds16o<SL + 8192, IM>(x1, voff, db);
  glds16o<SL + 24576, IM>(wi, voff, db);
}
template <int J> __device__ __forceinline__ void c_issue(const char* cr, const char* ci, unsigned voff, unsigned db) {
  glds16o<J * 16384, J * 128>(cr, voff, db);
  glds16o<J * 16384 + 8192, J * 128>(ci, voff, db);
}

__device__ __forceinline__ void partB(const Params& P, int chunk, int grp, lptr lds, u64* stamps) {
  const int tid = threadIdx.x, lane = tid & 63, wid = __builtin_amdgcn_readfirstlane(tid >> 6);
  const int tq = wid >> 1, pq = wid & 1, r32 = lane & 31, hh = lane >> 5;
  const int wrow = pq * 32 + 8 * ((r32 >> 2) & 3) + 4 * (r32 >> 4) + (r32 & 3);
  unsigned* epw = (unsigned*)(P.ws + WS_EPOCH);
  unsigned lre, lim, ldt, ep;
  {
    unsigned off = (unsigned)((grp * 64 + wrow) * 2), z = 0;
    asm volatile("global_load_ushort %0, %1, %2" : "=v"(lre) : "v"(off), "s"(P.Lre) : "memory");
    asm volatile("global_load_ushort %0, %1, %2" : "=v"(lim) : "v"(off), "s"(P.Lim) : "memory");
    asm volatile("global_load_ushort %0, %1, %2" : "=v"(ldt) : "v"(off), "s"(P.ldt_b) : "memory");
    asm volatile("global_load_dword %0, %1, %2 sc1" : "=v"(ep) : "v"(z), "s"(epw) : "memory");
  }
  const char* x0 = (const char*)P.x + (size_t)chunk * 128 * ROWB;
  const char* x1 = x0 + 64 * ROWB;
  const char* wr = (const char*)P.Bre + (size_t)grp * 64 * ROWB;
  const char* wi = (const char*)P.Bim + (size_t)grp * 64 * ROWB;
  const char* cr = (const char*)P.Cre + (size_t)grp * 64 * ROWB;
  const char* ci = (const char*)P.Cim + (size_t)grp * 64 * ROWB;
  const unsigned voff = (unsigned)((8 * wid + (lane >> 3)) * ROWB + (((lane & 7) ^ (4 * (wid & 1) + (lane >> 4))) << 4));
  const unsigned db = (unsigned)(uintptr_t)lds + wid * 1024;
  b_issue<0>(x0, x1, wr, wi, voff, db); b_issue<1>(x0, x1, wr, wi, voff, db); b_issue<2>(x0, x1, wr, wi, voff, db);
  b_issue<3>(x0, x1, wr, wi, voff, db); b_issue<4>(x0, x1, wr, wi, voff, db);
  const unsigned voff5 = voff + 640;
  asm volatile("s_waitcnt vmcnt(20)" : "+v"(lre), "+v"(lim), "+v"(ldt), "+v"(ep)::"memory");
  STAMP(1);
  const unsigned tag = ep + 1u;
  const int run = 2 * tq + hh, pl = pq * 32 + r32;
  C2 lamd, cfd, Q[8], W2, GW;
  C1 lam8, lam16, lam64;
  int c0 = 4 * tq + hh, c1 = c0 + 2;
  {
    const float lr = bflo(lre), li = bflo(lim), dt = rnd_bf16(exp_acc(bflo(ldt)));
    const float zr = dt * lr, zi = dt * li, l2 = zr * LOG2E, rv = zi * INV2PI;
    const float m = exp_acc(zr), th = __builtin_amdgcn_fractf(rv);
    const C1 lam{m * __builtin_amdgcn_cosf(th), m * __builtin_amdgcn_sinf(th)};
    const float inv = 1.f / (lr * lr + li * li);
    const C1 cf = cmul(C1{lam.r - 1.f, lam.i}, C1{lr * inv, -li * inv});
    lamd = C2{dup(lam.r), dup(lam.i)}; cfd = C2{dup(cf.r), dup(cf.i)};
    Q[0] = cmul(cfd, lamd);
#pragma unroll
    for (int k = 1; k < 8; ++k) Q[k] = cmul(Q[k - 1], lamd);
    lam8 = cexp1(8.f * l2, 8.f * rv); lam16 = cexp1(16.f * l2, 16.f * rv); lam64 = cexp1(64.f * l2, 64.f * rv);
    const f32x2 n2 = {(float)(16 * run), (float)(16 * run + 8)};
    W2 = cexp2(n2 * dup(l2), n2 * dup(rv));
    const f32x2 ng = {(float)(128 * max(chunk - 1 - c0, 0)), (float)(128 * max(chunk - 1 - c1, 0))};
    GW = cexp2(ng * dup(l2), ng * dup(rv));
    const f32x2 ok = {c0 < chunk ? 1.f : 0.f, c1 < chunk ? 1.f : 0.f};
    GW.r *= ok; GW.i *= ok;
    c0 = min(c0, chunk - 1); c1 = min(c1, chunk - 1);
  }
  const int n16 = lane & 15, g4 = lane >> 4;
  bf16x8 S0, S1;
  {
    const unsigned one = (n16 & 1) ? 0x3f800000u : 0x3f80u;
    const int w = (n16 & 7) >> 1, gs = g4 - 2 * (n16 >> 3);
    const unsigned a = gs == 0 ? one : 0u, b = gs == 1 ? one : 0u;
    S0 = cat4(w == 0 ? a : 0u, w == 1 ? a : 0u, w == 2 ? a : 0u, w == 3 ? a : 0u);
    S1 = cat4(w == 0 ? b : 0u, w == 1 ? b : 0u, w == 2 ? b : 0u, w == 3 ? b : 0u);
  }
  const int xrow = tq * 32 + 16 * ((r32 >> 2) & 1) + 2 * (r32 >> 3) + ((r32 >> 1) & 1) + 8 * (r32 & 1);
  const unsigned xa = xrow * 128 + ((hh ^ ((xrow >> 1) & 7)) << 4);
  const unsigned wa = 16384 + wrow * 128 + ((hh ^ ((wrow >> 1) & 7)) << 4);
  asm volatile("" : "+v"(lamd.r), "+v"(lamd.i), "+v"(cfd.r), "+v"(cfd.i), "+v"(W2.r), "+v"(W2.i), "+v"(GW.r), "+v"(GW.i), "+v"(S0), "+v"(S1));
  asm volatile("" : "+v"(Q[0].r), "+v"(Q[0].i), "+v"(Q[1].r), "+v"(Q[1].i), "+v"(Q[2].r), "+v"(Q[2].i), "+v"(Q[3].r), "+v"(Q[3].i),
               "+v"(Q[4].r), "+v"(Q[4].i), "+v"(Q[5].r), "+v"(Q[5].i), "+v"(Q[6].r), "+v"(Q[6].i), "+v"(Q[7].r), "+v"(Q[7].i));
  asm volatile("" : "+v"(lam8.r), "+v"(lam8.i), "+v"(lam16.r), "+v"(lam16.i), "+v"(lam64.r), "+v"(lam64.i), "+v"(c0), "+v"(c1));
  f32x16 are = {}, aim = {};
  STAMP(2);
  {
    bf16x8 fa[4], fb[4], fc[4];
#pragma unroll
    for (int s = 0; s < 9; ++s) {
      SB();
      if (s == 0) waitv_bar<16>(); else if (s <= 5) waitv_bar<12>(); else if (s <= 7) waitv_bar<10>(); else waitv_bar<6>();
      lptr st = lds + (s % 5) * 32768;
#pragma unroll
      for (int ks = 0; ks < 2; ++ks) {
        fa[ks] = lds128(st + (xa ^ (ks << 5))); fb[ks] = lds128(st + (wa ^ (ks << 5))); fc[ks] = lds128(st + (wa ^ (ks << 5)) + 8192);
      }
      SB();
      if (s == 1) b_issue<5>(x0, x1, wr, wi, voff5, db);
      if (s == 2) b_issue<6>(x0, x1, wr, wi, voff5, db);
      if (s == 3) b_issue<7>(x0, x1, wr, wi, voff5, db);
      if (s == 4) b_issue<8>(x0, x1, wr, wi, voff5, db);
      if (s == 5) c_issue<8>(cr, ci, voff, db);
      if (s == 6) { c_issue<0>(cr, ci, voff, db); c_issue<1>(cr, ci, voff, db); }
      if (s > 0) {
#pragma unroll
        for (int ks = 2; ks < 4; ++ks) { are = MFMA32(fa[ks], fb[ks], are); aim = MFMA32(fa[ks], fc[ks], aim); }
      }
      SB();
#pragma unroll
      for (int ks = 2; ks < 4; ++ks) {
        fa[ks] = lds128(st + (xa ^ (ks << 5))); fb[ks] = lds128(st + (wa ^ (ks << 5))); fc[ks] = lds128(st + (wa ^ (ks << 5)) + 8192);
      }
      SB();
#pragma unroll
      for (int ks = 0; ks < 2; ++ks) { are = MFMA32(fa[ks], fb[ks], are); aim = MFMA32(fa[ks], fc[ks], aim); }
    }
    SB();
#pragma unroll
    for (int ks = 2; ks < 4; ++ks) { are = MFMA32(fa[ks], fb[ks], are); aim = MFMA32(fa[ks], fc[ks], aim); }
  }
  STAMP(3);
  f32x2 gr[8], gi[8];
  gr[0] = f32x2{are[0], are[1]}; gi[0] = f32x2{aim[0], aim[1]};
#pragma unroll
  for (int k = 1; k < 8; ++k) {
    const f32x2 re = {are[2 * k], are[2 * k + 1]}, im = {aim[2 * k], aim[2 * k + 1]};
    gr[k] = PFMA(-lamd.i, gi[k - 1], PFMA(lamd.r, gr[k - 1], re));
    gi[k] = PFMA(lamd.i, gr[k - 1], PFMA(lamd.r, gi[k - 1], im));
  }
  lptr ex = lds + B_EX;
  const C1 sA{gr[7].x, gi[7].x};
  {
    const C1 s = cfma(lam8, sA, C1{gr[7].y, gi[7].y});
    *(LAS a_f32x2*)(ex + (run * 64 + pl) * 8) = f32x2{s.r, s.i};
  }
  BAR();
  const __amdgpu_buffer_rsrc_t grs = __builtin_amdgcn_make_buffer_rsrc(P.ws + WS_GRAN + grp * 16384, 0, 16384, 0x00020000);
  auto rd = [&](int o) { f32x2 v = *(LAS a_f32x2*)(ex + o); return C1{v.x, v.y}; };
  C1 lp{0.f, 0.f};
  if (tq == 0) {
    const int o = (4 * hh * 64 + pl) * 8;
    const C1 a = rd(o), b = rd(o + 512), c = rd(o + 1024), d = rd(o + 1536);
    const C1 s0 = rd(pl * 8);
    C1 t = cfma(lam16, cfma(lam16, cfma(lam16, a, b), c), d);
    const auto xr = __builtin_amdgcn_permlane32_swap(asu(t.r), asu(t.r), false, false);
    const auto xi = __builtin_amdgcn_permlane32_swap(asu(t.i), asu(t.i), false, false);
    const unsigned r0 = xr[0], r1 = xr[1], i0 = xi[0], i1 = xi[1];
    t = cfma(lam64, C1{asf(r0), asf(i0)}, C1{asf(r1), asf(i1)});
    if (hh == 0 && chunk < 15) {
      const u32x4 g = {asu(t.r), tag, asu(t.i), tag};
      __builtin_amdgcn_raw_buffer_store_b128(g, grs, (chunk * 64 + pl) * 16, 0, 16);
    }
    if (hh) lp = s0;
  } else {
    const int o = pl * 8;
    lp = cfma(lam16, rd(o), rd(o + 512));
    for (int r = 2; r < 2 * tq; r += 2) lp = cfma(lam16, cfma(lam16, lp, rd(o + r * 512)), rd(o + r * 512 + 512));
    const C1 e = cfma(lam16, lp, rd(o + 2 * tq * 512));
    if (hh) lp = e;
  }
  STAMP(4);
  C2 lp2;
  {
    const C1 e = cfma(lam8, lp, sA);
    lp2 = C2{f32x2{lp.r, e.r}, f32x2{lp.i, e.i}};
#pragma unroll
    for (int k = 0; k < 8; ++k) {
      const f32x2 a = PFMA(-cfd.i, gi[k], cfd.r * gr[k]), b = PFMA(-cfd.i, gr[k], -cfd.r * gi[k]);
      gr[k] = a; gi[k] = b;
    }
    asm volatile("" : "+v"(gr[0]), "+v"(gr[1]), "+v"(gr[2]), "+v"(gr[3]), "+v"(gr[4]), "+v"(gr[5]), "+v"(gr[6]), "+v"(gr[7]),
                 "+v"(gi[0]), "+v"(gi[1]), "+v"(gi[2]), "+v"(gi[3]), "+v"(gi[4]), "+v"(gi[5]), "+v"(gi[6]), "+v"(gi[7]), "+v"(lp2.r), "+v"(lp2.i));
  }
  SB();
  c_issue<2>(cr, ci, voff, db); c_issue<3>(cr, ci, voff, db); c_issue<4>(cr, ci, voff, db); c_issue<5>(cr, ci, voff, db);
  STAMP(5);
  {
    f32x2 part = {0.f, 0.f};
    if (4 * tq < chunk) {
      u32x4 g0, g1;
      for (unsigned spin = 0; spin < 100000u; ++spin) {
        g0 = __builtin_amdgcn_raw_buffer_load_b128(grs, (c0 * 64 + pl) * 16, 0, 16);
        g1 = __builtin_amdgcn_raw_buffer_load_b128(grs, (c1 * 64 + pl) * 16, 0, 16);
        const bool ok = g0.y == tag && g0.w == tag && g1.y == tag && g1.w == tag;
        if (__all(ok)) break;
        asm volatile("" ::: "memory");
      }
      const C1 t = cfma(C1{asf(g1.x), asf(g1.z)}, C1{GW.r.y, GW.i.y}, cmul(C1{asf(g0.x), asf(g0.z)}, C1{GW.r.x, GW.i.x}));
      part = f32x2{t.r, t.i};
    }
    *(LAS a_f32x2*)(ex + 4096 + (run * 64 + pl) * 8) = part;
  }
  const int trow = chunk * 128 + tq * 32 + 16 * (n16 >> 3) + 4 * pq + ((n16 & 7) >> 1) + 8 * (n16 & 1), dcol = grp * 64 + 4 * g4;
  u32x2 xv[4], dv[4];
  c_issue<6>(cr, ci, voff, db); c_issue<7>(cr, ci, voff, db);
  {
    const unsigned xo = (unsigned)(trow * ROWB + dcol * 2), dO = (unsigned)(dcol * 2);
#pragma unroll
    for (int dt = 0; dt < 4; ++dt) {
      asm volatile("global_load_dwordx2 %0, %1, %2 offset:%3" : "=v"(xv[dt]) : "v"(xo), "s"(P.x), "i"(dt * 32) : "memory");
      asm volatile("global_load_dwordx2 %0, %1, %2 offset:%3" : "=v"(dv[dt]) : "v"(dO), "s"(P.dsk_b), "i"(dt * 32) : "memory");
    }
  }
  BAR();
  STAMP(6);
  bf16x8 hf[2][2];
  {
    f32x2 H = *(LAS a_f32x2*)(ex + 4096 + pl * 8);
#pragma unroll
    for (int w = 1; w < 8; ++w) H += *(LAS a_f32x2*)(ex + 4096 + (w * 64 + pl) * 8);
    const f32x2 Hr = dup(H.x), Hi = dup(H.y);
    const f32x2 Er = PFMA(-W2.i, Hi, PFMA(W2.r, Hr, lp2.r)), Ei = PFMA(W2.i, Hr, PFMA(W2.r, Hi, lp2.i));
    unsigned hr[8], hi[8];
#pragma unroll
    for (int k = 0; k < 8; ++k) {
      const f32x2 a = PFMA(-Q[k].i, Ei, PFMA(Q[k].r, Er, gr[k])), b = PFMA(-Q[k].i, Er, PFMA(-Q[k].r, Ei, gi[k]));
      hr[k] = cvtpk(a.x, a.y); hi[k] = cvtpk(b.x, b.y);
    }
    const f32x4 z4 = {0.f, 0.f, 0.f, 0.f};
#pragma unroll
    for (int ts = 0; ts < 2; ++ts) {
      const bf16x8 ar = cat4(hr[4 * ts], hr[4 * ts + 1], hr[4 * ts + 2], hr[4 * ts + 3]);
      const bf16x8 ai = cat4(hi[4 * ts], hi[4 * ts + 1], hi[4 * ts + 2], hi[4 * ts + 3]);
      const f32x4 d0 = MFMA16(ar, S0, z4), d1 = MFMA16(ar, S1, z4), d2 = MFMA16(ai, S0, z4), d3 = MFMA16(ai, S1, z4);
      hf[0][ts] = cat4(cvtpk(d0[0], d0[1]), cvtpk(d0[2], d0[3]), cvtpk(d1[0], d1[1]), cvtpk(d1[2], d1[3]));
      hf[1][ts] = cat4(cvtpk(d2[0], d2[1]), cvtpk(d2[2], d2[3]), cvtpk(d3[0], d3[1]), cvtpk(d3[2], d3[3]));
    }
  }
  const int hset = (chunk * 4 + tq) * 2 + pq;
  const __amdgpu_buffer_rsrc_t hrs = __builtin_amdgcn_make_buffer_rsrc(P.ws + WS_H + (size_t)hset * 36864, 0, 36864, 0x00020000);
#pragma unroll
  for (int c = 0; c < 2; ++c)
#pragma unroll
    for (int ts = 0; ts < 2; ++ts)
      __builtin_amdgcn_raw_buffer_store_b128(__builtin_bit_cast(u32x4, hf[c][ts]), hrs, lane * 16 + (c * 2 + ts) * 1024, grp * 4096, 16);
  STAMP(7);
  asm volatile("s_waitcnt vmcnt(0)" : "+v"(xv[0]), "+v"(xv[1]), "+v"(xv[2]), "+v"(xv[3]), "+v"(dv[0]), "+v"(dv[1]), "+v"(dv[2]), "+v"(dv[3])::"memory");
  const __amdgpu_buffer_rsrc_t frs = __builtin_amdgcn_make_buffer_rsrc(P.ws + WS_FLAG + hset * 128, 0, 128, 0x00020000);
  if (lane == 0) __builtin_amdgcn_raw_buffer_store_b64(u32x2{tag, ~tag}, frs, grp * 8, 0, 16);
  unsigned warm;
  {
    const unsigned o = (lane & 31) * 128;
    const char* own = P.ws + WS_H + (size_t)hset * 36864 + grp * 4096;
    asm volatile("global_load_dword %0, %1, %2 sc1" : "=v"(warm) : "v"(o), "s"(own) : "memory");
  }
  STAMP(8);
  BAR();
  const unsigned ca = n16 * 128 + (((pq * 4 + g4) ^ (n16 >> 1)) << 4);
  f32x4 acc[2][4];
  {
    lptr cb = lds + ca + grp * 16384;
    bf16x8 cf[2][4];
#pragma unroll
    for (int c = 0; c < 2; ++c)
#pragma unroll
      for (int dt = 0; dt < 4; ++dt) cf[c][dt] = lds128(cb + c * 8192 + dt * 2048);
    const f32x4 z4 = {0.f, 0.f, 0.f, 0.f};
    const bf16x8 k0 = pq ? hf[0][1] : hf[0][0], s0 = pq ? hf[0][0] : hf[0][1], k1 = pq ? hf[1][1] : hf[1][0], s1 = pq ? hf[1][0] : hf[1][1];
#pragma unroll
    for (int dt = 0; dt < 4; ++dt) { acc[0][dt] = MFMA16(cf[0][dt], k0, z4); acc[1][dt] = MFMA16(cf[0][dt], s0, z4); }
#pragma unroll
    for (int dt = 0; dt < 4; ++dt) { acc[0][dt] = MFMA16(cf[1][dt], k1, acc[0][dt]); acc[1][dt] = MFMA16(cf[1][dt], s1, acc[1][dt]); }
  }
  SB();
  BAR();
  STAMP(9);
  {
    const int fo = (lane < 8 ? lane : 8) * 8;
    for (unsigned spin = 0; spin < 100000u; ++spin) {
      const u32x2 f = __builtin_amdgcn_raw_buffer_load_b64(frs, fo, 0, 16);
      if (__all(f.x == tag && f.y == ~tag)) break;
      asm volatile("" ::: "memory");
    }
    asm volatile("" :: "v"(warm) : "memory");
  }
  STAMP(10);
  if (chunk == 15 && grp == 0 && tid == 0) __hip_atomic_store((GAS unsigned*)epw, tag, __ATOMIC_RELAXED, __HIP_MEMORY_SCOPE_AGENT);
  {
    u32x4 hv[8][2][2];
    int jj[8];
    const int v0 = lane * 16 + pq * 1024, v1 = lane * 16 + (1 - pq) * 1024;
#pragma unroll
    for (int k = 0; k < 8; ++k) {
      jj[k] = grp + 1 + k; if (jj[k] >= 9) jj[k] -= 9;
#pragma unroll
      for (int c = 0; c < 2; ++c) {
        hv[k][c][0] = __builtin_amdgcn_raw_buffer_load_b128(hrs, v0 + c * 2048, jj[k] * 4096, 16);
        hv[k][c][1] = __builtin_amdgcn_raw_buffer_load_b128(hrs, v1 + c * 2048, jj[k] * 4096, 16);
      }
    }
    bf16x8 cf[2][4];
    {
      lptr cb = lds + ca + jj[0] * 16384;
#pragma unroll
      for (int dt = 0; dt < 4; ++dt) cf[0][dt] = lds128(cb + dt * 2048);
    }
#pragma unroll
    for (int un = 0; un < 16; ++un) {
      SB();
      if (un < 15) {
        lptr cb = lds + ca + jj[(un + 1) >> 1] * 16384 + ((un + 1) & 1) * 8192;
#pragma unroll
        for (int dt = 0; dt < 4; ++dt) cf[(un + 1) & 1][dt] = lds128(cb + dt * 2048);
      }
      SB();
      const bf16x8 h0 = __builtin_bit_cast(bf16x8, hv[un >> 1][un & 1][0]), h1 = __builtin_bit_cast(bf16x8, hv[un >> 1][un & 1][1]);
#pragma unroll
      for (int dt = 0; dt < 4; ++dt) { acc[0][dt] = MFMA16(cf[un & 1][dt], h0, acc[0][dt]); acc[1][dt] = MFMA16(cf[un & 1][dt], h1, acc[1][dt]); }
    }
  }
  STAMP(11);
  {
    const int w2 = wid ^ 1;
    lptr mine = lds + (wid < 4 ? grp * 16384 + wid * 4096 : B_EX + (wid - 4) * 4096) + lane * 16;
    lptr other = lds + (w2 < 4 ? grp * 16384 + w2 * 4096 : B_EX + (w2 - 4) * 4096) + lane * 16;
#pragma unroll
    for (int dt = 0; dt < 4; ++dt) *(LAS a_f32x4*)(mine + dt * 1024) = acc[1][dt];
    float sk[16];
#pragma unroll
    for (int dt = 0; dt < 4; ++dt) {
      const unsigned s0 = cvtpk(bflo(xv[dt].x) * bflo(dv[dt].x), bfhi(xv[dt].x) * bfhi(dv[dt].x));
      const unsigned s1 = cvtpk(bflo(xv[dt].y) * bflo(dv[dt].y), bfhi(xv[dt].y) * bfhi(dv[dt].y));
      sk[4 * dt] = fmaf(2.f, acc[0][dt][0], bflo(s0)); sk[4 * dt + 1] = fmaf(2.f, acc[0][dt][1], bfhi(s0));
      sk[4 * dt + 2] = fmaf(2.f, acc[0][dt][2], bflo(s1)); sk[4 * dt + 3] = fmaf(2.f, acc[0][dt][3], bfhi(s1));
    }
    SB();
    BAR();
    float* op = P.out + (size_t)trow * (2 * D) + D + dcol;
#pragma unroll
    for (int dt = 0; dt < 4; ++dt) {
      const f32x4 o = *(LAS a_f32x4*)(other + dt * 1024);
      const f32x4 r = {fmaf(2.f, o[0], sk[4 * dt]), fmaf(2.f, o[1], sk[4 * dt + 1]), fmaf(2.f, o[2], sk[4 * dt + 2]), fmaf(2.f, o[3], sk[4 * dt + 3])};
      *(f32x4*)(op + dt * 16) = r;
    }
  }
  STAMP(12);
}

constexpr int CODE_BYTES = 16384;

__global__ __launch_bounds__(512, 2) void fused(Params P) {
  extern __shared__ __attribute__((aligned(16))) char smem[];
  lptr lds = (lptr)smem;
  const int xcd = blockIdx.x & 7, loc = blockIdx.x >> 3;
  u64 stamps[14], code;
  asm volatile("s_getpc_b64 %0" : "=s"(code));
  STAMP(0);
  if (loc >= 1 && loc < 19) { partB(P, 2 * xcd + (loc - 1) / 9, (loc - 1) % 9, lds, stamps); FLUSH(blockIdx.x, 2, 13); }
  else if (loc >= 19 && loc < 28) { partA(P, xcd * 9 + loc - 19, lds, stamps); FLUSH(blockIdx.x, 1, 9); }
  else {
    const int lane = threadIdx.x & 63, wid = __builtin_amdgcn_readfirstlane(threadIdx.x >> 6);
    const unsigned lds0 = (unsigned)(uintptr_t)lds;
    const int hid = loc == 0 ? 0 : loc - 27;
    if (hid == 0) {
      code &= ~(u64)255;
      for (int i = wid; i < CODE_BYTES / 1024; i += 8) glds16((const char*)code + i * 1024, lane * 16, lds0 + wid * 2048);
    }
    for (int i = hid * 8 + wid; i < 648; i += 40) {
      glds16((const char*)P.Cre + i * 1024, lane * 16, lds0 + wid * 2048);
      glds16((const char*)P.Cim + i * 1024, lane * 16, lds0 + wid * 2048 + 1024);
    }
    WAITV(0);
    STAMP(1);
    FLUSH(blockIdx.x, 3, 2);
  }
}

extern "C" void kernel_launch(void* const* d_in, const int* in_sizes, int n_in, void* d_out, int out_size, void* d_ws,
                              size_t ws_size, hipStream_t stream) {
  Params P;
  P.u = (const u16*)d_in[0]; P.Are = (const u16*)d_in[1]; P.Aim = (const u16*)d_in[2]; P.Bre_a = (const u16*)d_in[3];
  P.Bim_a = (const u16*)d_in[4]; P.Cre_a = (const u16*)d_in[5]; P.Cim_a = (const u16*)d_in[6]; P.ldt_a = (const u16*)d_in[7];
  P.dsk_a = (const u16*)d_in[8]; P.x = (const u16*)d_in[9]; P.Lre = (const u16*)d_in[10]; P.Lim = (const u16*)d_in[11];
  P.Bre = (const u16*)d_in[12]; P.Bim = (const u16*)d_in[13]; P.Cre = (const u16*)d_in[14]; P.Cim = (const u16*)d_in[15];
  P.ldt_b = (const u16*)d_in[16]; P.dsk_b = (const u16*)d_in[17];
  P.out = (float*)d_out; P.ws = (char*)d_ws;
  (void)hipFuncSetAttribute((const void*)fused, hipFuncAttributeMaxDynamicSharedMemorySize, 163840);
  fused<<<256, 512, 163840, stream>>>(P);
}
```

```cpp
#include <hip/hip_runtime.h>
#include <stdint.h>

#define LAS __attribute__((address_space(3)))
#define GAS __attribute__((address_space(1)))
typedef short bf16x8 __attribute__((ext_vector_type(8)));
typedef short s16x4 __attribute__((ext_vector_type(4)));
typedef float f32x2 __attribute__((ext_vector_type(2)));
typedef float f32x4 __attribute__((ext_vector_type(4)));
typedef float f32x16 __attribute__((ext_vector_type(16)));
typedef unsigned u32x2 __attribute__((ext_vector_type(2)));
typedef unsigned u32x3 __attribute__((ext_vector_type(3)));
typedef unsigned u32x4 __attribute__((ext_vector_type(4)));
typedef __bf16 bf2 __attribute__((ext_vector_type(2)));
typedef unsigned long long u64;
typedef unsigned short u16;
typedef LAS char* lptr;
#define MA __attribute__((may_alias))
typedef bf16x8 MA a_bf16x8; typedef s16x4 MA a_s16x4; typedef f32x2 MA a_f32x2; typedef f32x4 MA a_f32x4;
typedef u32x2 MA a_u32x2; typedef u32x4 MA a_u32x4; typedef unsigned MA a_u32; typedef u16 MA a_u16;

#define MFMA16(a, b, c) __builtin_amdgcn_mfma_f32_16x16x32_bf16(a, b, c, 0, 0, 0)
#define MFMA32(a, b, c) __builtin_amdgcn_mfma_f32_32x32x16_bf16(a, b, c, 0, 0, 0)

#define SB() __builtin_amdgcn_sched_barrier(0)

constexpr int L = 2048, D = 576, ROWB = D * 2;
constexpr size_t WS_H = 0, WS_GRAN = 8u << 20, WS_EPOCH = 9u << 20;

struct Params {
  const u16* u; const u16* Are; const u16* Aim; const u16* Bre_a; const u16* Bim_a; const u16* Cre_a; const u16* Cim_a;
  const u16* ldt_a; const u16* dsk_a;
  const u16* x; const u16* Lre; const u16* Lim; const u16* Bre; const u16* Bim; const u16* Cre; const u16* Cim;
  const u16* ldt_b; const u16* dsk_b;
  float* out; char* ws;
};

__device__ __forceinline__ unsigned cvtpk(float a, float b) {
  f32x2 v = {a, b};
  return __builtin_bit_cast(unsigned, __builtin_convertvector(v, bf2));
}
__device__ __forceinline__ float asf(unsigned v) { return __builtin_bit_cast(float, v); }
__device__ __forceinline__ unsigned asu(float v) { return __builtin_bit_cast(unsigned, v); }
__device__ __forceinline__ float bflo(unsigned v) { return __builtin_bit_cast(float, v << 16); }
__device__ __forceinline__ float bfhi(unsigned v) { return __builtin_bit_cast(float, v & 0xffff0000u); }
__device__ __forceinline__ float rnd_bf16(float v) { return bflo(cvtpk(v, v)); }
struct C1 { float r, i; };
__device__ __forceinline__ float opq(float v) { asm("" : "+v"(v)); return v; }
__device__ __forceinline__ C1 cmul(C1 a, C1 b) { return C1{opq(fmaf(a.r, b.r, -(a.i * b.i))), opq(fmaf(a.r, b.i, a.i * b.r))}; }
__device__ __forceinline__ C1 cfma(C1 a, C1 b, C1 c) { return C1{opq(fmaf(-a.i, b.i, fmaf(a.r, b.r, c.r))), opq(fmaf(a.i, b.r, fmaf(a.r, b.i, c.i)))}; }
struct C2 { f32x2 r, i; };
#define PFMA __builtin_elementwise_fma
__device__ __forceinline__ C2 cmul(const C2& a, const C2& e) { C2 o; f32x2 t0 = e.r * a.r, t1 = e.r * a.i; o.r = PFMA(-e.i, a.i, t0); o.i = PFMA(e.i, a.r, t1); return o; }
__device__ __forceinline__ f32x2 dup(float v) { f32x2 d = {v, v}; asm("" : "+v"(d)); return d; }
__device__ __forceinline__ C1 cexp1(float l2, float rev) {
  float m = __builtin_amdgcn_exp2f(l2), th = __builtin_amdgcn_fractf(rev);
  return C1{m * __builtin_amdgcn_cosf(th), m * __builtin_amdgcn_sinf(th)};
}
__device__ __forceinline__ C2 cexp2(f32x2 l2, f32x2 rev) {
  float x0 = l2.x, x1 = l2.y, t0 = __builtin_amdgcn_fractf(rev.x), t1 = __builtin_amdgcn_fractf(rev.y);
  f32x2 m = {__builtin_amdgcn_exp2f(x0), __builtin_amdgcn_exp2f(x1)};
  f32x2 c = {__builtin_amdgcn_cosf(t0), __builtin_amdgcn_cosf(t1)}, s = {__builtin_amdgcn_sinf(t0), __builtin_amdgcn_sinf(t1)};
  return C2{m * c, m * s};
}
__device__ __forceinline__ float exp_acc(float x) {
  float hi = x * 1.4426950216293335f;
  float lo = fmaf(x, 1.925963033500011e-8f, fmaf(x, 1.4426950216293335f, -hi));
  float m = __builtin_amdgcn_exp2f(hi);
  return fmaf(m, lo * 0.6931471805599453f, m);
}
constexpr float LOG2E = 1.4426950408889634f, INV2PI = 0.15915494309189535f;
__device__ __forceinline__ void glds16(const void* sbase, unsigned voff, unsigned dst) {
  asm volatile("s_mov_b32 m0, %2\n\ts_nop 0\n\tglobal_load_lds_dwordx4 %0, %1" ::"v"(voff), "s"(sbase), "s"(dst) : "memory");
}
template <int DST, int IMM> __device__ __forceinline__ void glds16o(const void* sbase, unsigned voff, unsigned dbase) {
  asm volatile("s_add_i32 m0, %2, %3\n\ts_nop 0\n\tglobal_load_lds_dwordx4 %0, %1 offset:%4" ::"v"(voff), "s"(sbase), "s"(dbase), "i"(DST - IMM), "i"(IMM) : "memory", "scc");
}
template <class T> __device__ __forceinline__ void st16a(const void* sbase, unsigned voff, T v) {
  asm volatile("global_store_dwordx4 %0, %1, %2\n\ts_nop 1" ::"v"(voff), "v"(v), "s"(sbase) : "memory");
}
template <int IMM, class T> __device__ __forceinline__ void st16(const void* sbase, unsigned voff, T v) {
  asm volatile("global_store_dwordx4 %0, %1, %2 offset:%3 sc1\n\ts_nop 1" ::"v"(voff), "v"(v), "s"(sbase), "i"(IMM) : "memory");
}
__device__ __forceinline__ void touch(const void* sbase, unsigned voff, unsigned dst) {
  asm volatile("s_mov_b32 m0, %2\n\ts_nop 0\n\tglobal_load_lds_dword %0, %1" ::"v"(voff), "s"(sbase), "s"(dst) : "memory");
}
__device__ __forceinline__ void b_touch(const Params& P, int xcd, int q, int lane, unsigned dst) {
  const int s = (q * 745) >> 14, r = q - 22 * s;
  const char* src = r < 4 ? (const char*)P.x + (xcd * 4 + r) * 64 * ROWB : (const char*)((r & 1) ? P.Bim : P.Bre) + ((r - 4) >> 1) * 64 * ROWB;
  touch(src + (s + 2) * 128, lane * ROWB, dst);
}
__device__ __forceinline__ void c_touch(const Params& P, int q, int lane, unsigned dst) {
  const int i = (q * 3641) >> 16, r = q - 18 * i, g = r >> 1;
  int k = g + i; if (k >= 9) k -= 9;
  touch((const char*)((r & 1) ? P.Cim : P.Cre) + g * 64 * ROWB + k * 128, lane * ROWB, dst);
}
#define PIN1(a) asm volatile("" : "+v"(a))
#define PIN2(a, b) asm volatile("" : "+v"(a), "+v"(b))
#define PIN4(a, b, c, d) asm volatile("" : "+v"(a), "+v"(b), "+v"(c), "+v"(d))
#define WAITV(n) asm volatile("s_waitcnt vmcnt(" #n ")" ::: "memory")
#define BAR() asm volatile("s_waitcnt lgkmcnt(0)\n\ts_barrier" ::: "memory")
template <int N> __device__ __forceinline__ void waitv_bar() { asm volatile("s_waitcnt vmcnt(%0) lgkmcnt(0)\n\ts_barrier" ::"i"(N) : "memory"); }
__device__ __forceinline__ bf16x8 lds128(lptr p) { return *(LAS a_bf16x8*)p; }
__device__ __forceinline__ s16x4 ldstr(lptr p) { return __builtin_amdgcn_ds_read_tr16_b64_v4i16((LAS s16x4*)p); }
__device__ __forceinline__ bf16x8 cat(u32x2 a, u32x2 b) { u32x4 v = {a.x, a.y, b.x, b.y}; return __builtin_bit_cast(bf16x8, v); }
__device__ __forceinline__ bf16x8 cat4(unsigned a, unsigned b, unsigned c, unsigned d) { u32x4 v = {a, b, c, d}; return __builtin_bit_cast(bf16x8, v); }


constexpr int A_REG = 16384;
constexpr int A_STG = 8 * A_REG;
constexpr int A_R1 = 2112, A_UT = 4352;
constexpr int SQ_N = 0, SQ_M = 560, SQ_F = 560 + 4 * 64;
typedef const __attribute__((address_space(4))) unsigned* cptr;

__device__ __forceinline__ u32x2 ld2(lptr p) { u32x2 r; r.x = *(LAS a_u32*)p; r.y = *(LAS a_u32*)(p + 4); return r; }

__device__ __forceinline__ bf16x8 band(int lane, int ctr, int h) {
  int base = (lane & 15) + ctr - 8 * (lane >> 4);
  unsigned w[4];
#pragma unroll
  for (int j = 0; j < 4; ++j) {
    int d0 = base - 2 * j, d1 = d0 - 1;
    unsigned lo = ((unsigned)(d0 + h) <= (unsigned)(2 * h)) ? 0x3f80u : 0u;
    unsigned hi = ((unsigned)(d1 + h) <= (unsigned)(2 * h)) ? 0x3f800000u : 0u;
    w[j] = lo | hi;
  }
  return cat4(w[0], w[1], w[2], w[3]);
}

__device__ __forceinline__ void partA(const Params& P, int cg, int tq0, lptr lds) {
  const int tid = threadIdx.x, lane = tid & 63, wid = __builtin_amdgcn_readfirstlane(tid >> 6);
  const int ch = cg * 8 + wid;
  const unsigned lds0 = (unsigned)(uintptr_t)lds;
  const unsigned ldw = ((cptr)P.ldt_a)[ch >> 1], dsw = ((cptr)P.dsk_a)[ch >> 1];
  lptr reg = lds + wid * A_REG;
  const int a16 = lane & 15, g4 = lane >> 4, r32 = lane & 31, hh = lane >> 5;
  bf16x8 b12_1 = band(lane, 12, 1), b8_2 = band(lane, 8, 2), b4_1 = band(lane, 4, 1), b8_3 = band(lane, 8, 3), b8_4 = band(lane, 8, 4);
  lptr wr[16], rd[8];
#pragma unroll
  for (int r = 0; r < 16; ++r) wr[r] = reg + ((lane * 8) ^ (r << 4));
#pragma unroll
  for (int k = 0; k < 8; ++k) rd[k] = reg + r32 * 512 + (((hh ^ (r32 & 15)) << 4) ^ (k << 5));
  const float dt = rnd_bf16(exp_acc((ch & 1) ? bfhi(ldw) : bflo(ldw)));
  const float dskip = (ch & 1) ? bfhi(dsw) : bflo(dsw);
  f32x2 dt2 = dup(dt), dl2 = dup(LOG2E), dr2 = dup(INV2PI), c8 = dup(8.f), c256 = dup(256.f);
  const char* ucol = (const char*)P.u + cg * 16;
  const unsigned uoff = (unsigned)((wid * 256 + lane) * ROWB), udst = lds0 + A_STG + wid * 4096;
  asm volatile("" : "+v"(b12_1), "+v"(b8_2), "+v"(b4_1), "+v"(b8_3), "+v"(b8_4), "+v"(dt2));
  asm volatile("" : "+v"(wr[0]), "+v"(wr[1]), "+v"(wr[2]), "+v"(wr[3]), "+v"(wr[4]), "+v"(wr[5]), "+v"(wr[6]), "+v"(wr[7]),
               "+v"(wr[8]), "+v"(wr[9]), "+v"(wr[10]), "+v"(wr[11]), "+v"(wr[12]), "+v"(wr[13]), "+v"(wr[14]), "+v"(wr[15]));
  asm volatile("" : "+v"(rd[0]), "+v"(rd[1]), "+v"(rd[2]), "+v"(rd[3]), "+v"(rd[4]), "+v"(rd[5]), "+v"(rd[6]), "+v"(rd[7]));
  WAITV(0);
  f32x2 pa[6];
#pragma unroll
  for (int i = 0; i < 6; ++i) { const unsigned v = *(LAS a_u32*)(reg + lane * 4 + i * 256); pa[i] = f32x2{bflo(v), bfhi(v)}; }
  b_touch(P, blockIdx.x & 7, tq0 + wid, lane, udst);
  f32x16 kacc = {};
  {
    auto put = [&](int row, const C2& v) { *(LAS a_u32x2*)(wr[row & 15] + row * 512) = u32x2{cvtpk(v.r.x, v.r.y), cvtpk(v.i.x, v.i.y)}; };
    auto udma = [&](int k) { const int p = (k + cg) & 3; glds16(ucol + p * 64 * ROWB, uoff, udst + p * 1024); };
    const f32x2 l2 = (dt2 * pa[0]) * dl2, rv = (dt2 * pa[1]) * dr2;
    const C2 e8 = cexp2(l2 * c8, rv * c8);
    const C2 W = cmul(C2{pa[4], pa[5]}, C2{dt2 * pa[2], dt2 * pa[3]});
    C2 x1 = cexp2(l2, rv), x256 = cexp2(l2 * c256, rv * c256);
    x1.i = -x1.i; x256.i = -x256.i;
    const C2 x8{e8.r, -e8.i};
    C2 p{W.r, -W.i};
    const C2 q256 = cmul(p, x256);
    C2 q260;
    put(0, p); put(8, q256);
#pragma unroll
    for (int a = 1; a < 8; ++a) {
      p = cmul(p, x1); put(a, p);
      if ((a & 1) == 0) { C2 t = cmul(p, x256); put(8 + a / 2, t); if (a == 4) q260 = t; }
    }
    const C2 q264 = cmul(q256, x8); put(16, q264);
    const C2 q516 = cmul(q260, x256), q520 = cmul(q264, x256); put(12, q516); put(13, q520);
    const C2 q772 = cmul(q516, x256);
    C2 q = cmul(q520, x256); put(14, q772); put(15, q);
#pragma unroll
    for (int k = 0; k < 4; ++k) { q = cmul(q, x256); put(20 + k, q); }
    SB();
    bf16x8 fa[16];
#pragma unroll
    for (int ks = 0; ks < 16; ++ks) fa[ks] = lds128(rd[ks & 7] + (ks >> 3) * 256);
    SB();
    *(LAS a_u32x2*)(wr[0]) = u32x2{0x3f803f80u, 0u};
    C2 g = e8;
    put(1, g);
#pragma unroll
    for (int c = 2; c < 32; ++c) {
      g = cmul(g, e8); put(c, g);
      if ((c & 7) == 2) udma(c >> 3);
    }
    SB();
    bf16x8 fb[16];
#pragma unroll
    for (int ks = 0; ks < 16; ++ks) fb[ks] = lds128(rd[ks & 7] + (ks >> 3) * 256);
    SB();
    const u32x4 z = {0u, 0u, 0u, 0u};
    *(LAS a_u32x4*)(reg + lane * 16) = z;
    *(LAS a_u32x4*)(reg + 1024 + lane * 16) = z;
    if (lane < 32) *(LAS a_u32x4*)(reg + A_UT + wid * 16 + lane * 16) = z;
    SB();
#pragma unroll
    for (int ks = 0; ks < 16; ++ks) kacc = MFMA32(fa[ks], fb[ks], kacc);
  }
  float k1024;
  {
    const int c = r32;
    float k0 = kacc[0];
    if (lane == 0) k0 += dskip;
    u32x2 nw = {cvtpk(kacc[3], kacc[2]), cvtpk(kacc[1], k0)};
    *(LAS a_u32x2*)(reg + (SQ_N + 540 - 8 * c - 4 * hh) * 2) = nw;
    if (hh == 0) {
      u32x4 mw = {cvtpk(kacc[8], kacc[7]), cvtpk(kacc[6], kacc[6]), cvtpk(kacc[6], kacc[5]), cvtpk(kacc[4], kacc[4])};
      *(LAS a_u32x4*)(reg + (SQ_N + 280 - 8 * c) * 2) = mw;
      if (c == 31) {
        unsigned v = cvtpk(kacc[8], kacc[8]);
        *(LAS a_u32*)(reg + (SQ_N + 30) * 2) = v;
        *(LAS a_u16*)(reg + (SQ_N + 29) * 2) = (u16)v;
      }
    } else {
      const int odd = c & 1, cm = c >> 1;
      lptr m0 = reg + (SQ_M + (2 * odd) * 64 + 79 - 32 - cm) * 2;
      unsigned a = cvtpk(kacc[4], kacc[5]), b = cvtpk(kacc[6], kacc[7]);
      *(LAS a_u16*)(m0) = (u16)a; *(LAS a_u16*)(m0 + 128) = (u16)(a >> 16);
      *(LAS a_u16*)(m0 - 32) = (u16)b; *(LAS a_u16*)(m0 + 128 - 32) = (u16)(b >> 16);
      lptr f0 = reg + (SQ_F + odd * 80 + 127 - 64 - cm) * 2;
      unsigned e = cvtpk(kacc[8], kacc[9]), f = cvtpk(kacc[10], kacc[11]);
      *(LAS a_u16*)(f0) = (u16)e; *(LAS a_u16*)(f0 - 32) = (u16)(e >> 16);
      *(LAS a_u16*)(f0 - 64) = (u16)f; *(LAS a_u16*)(f0 - 96) = (u16)(f >> 16);
    }
    k1024 = asf(__builtin_amdgcn_readlane(asu(kacc[7]), 63));
    u32x4 v[2]; unsigned nx[2];
#pragma unroll
    for (int it = 0; it < 2; ++it) { lptr s = reg + it * 1024 + lane * 16; v[it] = *(LAS a_u32x4*)s; nx[it] = *(LAS a_u32*)(s + 16); }
#pragma unroll
    for (int it = 0; it < 2; ++it) {
      u32x4 o = {__builtin_amdgcn_alignbit(v[it].y, v[it].x, 16), __builtin_amdgcn_alignbit(v[it].z, v[it].y, 16),
                 __builtin_amdgcn_alignbit(v[it].w, v[it].z, 16), __builtin_amdgcn_alignbit(nx[it], v[it].w, 16)};
      *(LAS a_u32x4*)(reg + it * 1024 + lane * 16 + A_R1) = o;
    }
  }
  bf16x8 KA[17], KM[3][2], KF[4], KC;
  {
    lptr kn = reg + ((a16 & 1) ? (527 - a16 + 8 * g4) * 2 : A_R1 + (526 - a16 + 8 * g4) * 2);
    lptr kf = reg + ((a16 & 1) ? (79 - a16 + 4 * g4) * 2 : A_R1 + (78 - a16 + 4 * g4) * 2);
#pragma unroll
    for (int dl = 2; dl <= 4; ++dl)
#pragma unroll
      for (int pr = 0; pr < 2; ++pr)
        KM[dl - 2][pr] = cat(ld2(kf + (SQ_M + (2 * pr) * 64) * 2 - 32 * dl), ld2(kf + (SQ_M + (2 * pr + 1) * 64) * 2 - 32 * dl));
#pragma unroll
    for (int dl = 4; dl <= 7; ++dl) KF[dl - 4] = cat(ld2(kf + (SQ_F + 48) * 2 - 32 * dl), ld2(kf + (SQ_F + 80 + 48) * 2 - 32 * dl));
#pragma unroll
    for (int q = 0; q < 17; ++q) KA[q] = cat(ld2(kn - 64 * q), ld2(kn - 64 * q + 8));
    unsigned kb = cvtpk(k1024, k1024), w[4];
    int j0 = a16 + 12 - 8 * g4;
#pragma unroll
    for (int j = 0; j < 4; ++j)
      w[j] = (((unsigned)(2 * j - j0) <= 1u) ? (kb & 0xffffu) : 0u) | (((unsigned)(2 * j + 1 - j0) <= 1u) ? (kb & 0xffff0000u) : 0u);
    KC = cat4(w[0], w[1], w[2], w[3]);
  }
  const int q4 = (lane >> 2) & 3, pp = lane & 3;
  lptr tsrc = lds + A_STG + (wid * 256 + g4 * 8 + (pp >> 1) * 4 + q4) * 16 + (pp & 1) * 8;
  lptr tdst = lds + (a16 & 7) * (A_REG + 16) + A_UT + 512 + (wid * 256 + g4 * 8 + (a16 >> 3) * 4) * 2;
  asm volatile("" : "+v"(tsrc), "+v"(tdst), "+v"(KC));
  SB();
  waitv_bar<0>();
  {
    s16x4 t[8];
#pragma unroll
    for (int k = 0; k < 8; ++k) t[k] = ldstr(tsrc + k * 512);
#pragma unroll
    for (int k = 0; k < 8; ++k) *(LAS a_s16x4*)(tdst + k * 64) = t[k];
  }
  SB();
  BAR();
  lptr ub = reg + A_UT + wid * 16 + 512 + a16 * 32 + g4 * 16;
  f32x4 acc[8];
  const f32x4 z4 = {0.f, 0.f, 0.f, 0.f};
#pragma unroll
  for (int j = 0; j < 8; ++j) acc[j] = z4;
  constexpr int LA = 4;
  bf16x8 F[64];
  {
    bf16x8 ua[6], uc[6];
#pragma unroll
    for (int i = 0; i < 6; ++i) { ua[i] = lds128(ub + 32 * (16 * i - 1)); uc[i] = lds128(ub + 32 * (16 * i - 1) - 16); }
#pragma unroll
    for (int n = 0; n < LA; ++n) F[n] = lds128(ub + 32 * (2 * n - 15));
    f32x4 d[2][6];
    auto box = [&](int i) {
      d[i & 1][0] = MFMA16(ua[i], b12_1, z4); d[i & 1][1] = MFMA16(ua[i], b8_2, z4);
      d[i & 1][2] = MFMA16(ua[i], b4_1, z4); d[i & 1][3] = MFMA16(uc[i], b8_2, z4);
      if (i < 4) { d[i & 1][4] = MFMA16(ua[i], b8_3, z4); d[i & 1][5] = MFMA16(uc[i], b8_4, z4); acc[i + 4] = MFMA16(KC, ua[i], acc[i + 4]); }
    };
    auto pk = [](const f32x4& a, const f32x4& b) { return cat4(cvtpk(a[0], a[1]), cvtpk(a[2], a[3]), cvtpk(b[0], b[1]), cvtpk(b[2], b[3])); };
    SB();
    box(0);
#pragma unroll
    for (int i = 0; i < 6; ++i) {
      SB();
      if (i < 5) box(i + 1);
      SB();
      const bf16x8 w0 = pk(d[i & 1][0], d[i & 1][1]), w1 = pk(d[i & 1][2], d[i & 1][3]);
      bf16x8 wf = w0;
      if (i < 4) wf = pk(d[i & 1][4], d[i & 1][5]);
#pragma unroll
      for (int dl = 2; dl <= 4; ++dl) if (i + dl < 8) acc[i + dl] = MFMA16(w0, KM[dl - 2][0], acc[i + dl]);
#pragma unroll
      for (int dl = 2; dl <= 4; ++dl) if (i + dl < 8) acc[i + dl] = MFMA16(w1, KM[dl - 2][1], acc[i + dl]);
      if (i < 4) {
#pragma unroll
        for (int dl = 7; dl >= 4; --dl) if (i + dl < 8) acc[i + dl] = MFMA16(wf, KF[dl - 4], acc[i + dl]);
      }
    }
  }
  {
    lptr ystg = lds + A_STG;
    lptr yw = ystg + wid * 4096 + 32 * (a16 ^ wid) + 8 * (g4 ^ (a16 >> 2));
    const int yc = (g4 & 1) * 4 + q4;
    lptr yr = ystg + yc * 4096 + 32 * ((2 * wid + (g4 >> 1)) ^ yc) + 8 * (pp ^ (wid >> 1));
    const unsigned oo = (unsigned)((wid * 32 + (g4 >> 1) * 16 + a16) * (8 * D) + (g4 & 1) * 16);
    const float* ob = P.out + cg * 8;
    u32x2 ty[2];
    auto out2 = [&](int j) {
#pragma unroll
      for (int k = 0; k < 2; ++k) {
        f32x4 o = {bflo(ty[k].x), bfhi(ty[k].x), bflo(ty[k].y), bfhi(ty[k].y)};
        st16a(ob + (j + k) * 256 * (2 * D), oo, o);
      }
    };
#pragma unroll
    for (int n = 0; n < 64; ++n) {
      SB();
      if (n + LA < 64) F[n + LA] = lds128(ub + 32 * (2 * (n + LA) - 15));
#pragma unroll
      for (int j = 0; j < 8; ++j) {
        const int q = 8 * j + 7 - n;
        if (q >= 0 && q <= 16) acc[j] = MFMA16(KA[q], F[n], acc[j]);
      }
      if ((n & 7) == 0 && n > 0) {
        const int j = (n >> 3) - 1;
        *(LAS a_u32x2*)(yw + j * 512) = u32x2{cvtpk(acc[j][0], acc[j][1]), cvtpk(acc[j][2], acc[j][3])};
      }
      if ((n & 15) == 2 && n > 2) {
        const int j = (n >> 3) - 2;
        SB(); BAR();
        ty[0] = __builtin_bit_cast(u32x2, ldstr(yr + j * 512)); ty[1] = __builtin_bit_cast(u32x2, ldstr(yr + j * 512 + 512));
      }
      if ((n & 15) == 5 && n > 5) out2((n >> 3) - 2);
    }
    SB();
    *(LAS a_u32x2*)(yw + 7 * 512) = u32x2{cvtpk(acc[7][0], acc[7][1]), cvtpk(acc[7][2], acc[7][3])};
    BAR();
    ty[0] = __builtin_bit_cast(u32x2, ldstr(yr + 6 * 512)); ty[1] = __builtin_bit_cast(u32x2, ldstr(yr + 7 * 512));
    out2(6);
  }
}

constexpr int B_EX = 147456;
constexpr int B_CNT = B_EX + 12288;
constexpr int B_SKIP = B_EX + 14848;
constexpr int H_BLK = 4224;
#define NV2 2
#define GLA 3
struct BSrc { const char *x0, *x1, *wr, *wi; unsigned vx, vw, voff, db; };
template <int S, int J> __device__ __forceinline__ void b_piece(const BSrc& b, unsigned vx, unsigned vw) {
  constexpr int SL = (S & 3) * 32768 + (J & 1) * 16384 + (J >> 1) * 8192, IM = (S & 3) * 128;
  glds16o<SL, IM>(J == 0 ? b.x0 : J == 1 ? b.wr : J == 2 ? b.x1 : b.wi, (J & 1) ? vw : vx, b.db);
}
template <int S> __device__ __forceinline__ void b_issue(const BSrc& b) { b_piece<S, 0>(b, b.vx, b.vw); b_piece<S, 1>(b, b.vx, b.vw); b_piece<S, 2>(b, b.vx, b.vw); b_piece<S, 3>(b, b.vx, b.vw); }
__device__ __forceinline__ unsigned c_voff(int grp, int J, unsigned voff) { int k = grp + 1 + J; if (k >= 9) k -= 9; return voff + k * 128; }
template <int J> __device__ __forceinline__ void c_issue(const char* cr, const char* ci, int grp, unsigned voff, unsigned db) {
  const unsigned v = c_voff(grp, J, voff);
  glds16o<J * 16384, 0>(cr, v, db);
  glds16o<J * 16384 + 8192, 0>(ci, v, db);
}
template <int N> __device__ __forceinline__ void waitvm_bar() { asm volatile("s_waitcnt vmcnt(%0)\n\ts_barrier" ::"i"(N) : "memory"); }
__device__ __forceinline__ C2 dup(C1 a) { return C2{dup(a.r), dup(a.i)}; }
__device__ __forceinline__ BSrc b_src(const Params& P, int chunk, int grp, lptr lds) {
  const int lane = threadIdx.x & 63, wid = __builtin_amdgcn_readfirstlane(threadIdx.x >> 6);
  BSrc b;
  b.x0 = (const char*)P.x + (size_t)chunk * 128 * ROWB; b.x1 = b.x0 + 64 * ROWB;
  b.wr = (const char*)P.Bre + (size_t)grp * 64 * ROWB; b.wi = (const char*)P.Bim + (size_t)grp * 64 * ROWB;
  b.voff = (unsigned)((8 * wid + (lane >> 3)) * ROWB + (((lane & 7) ^ (4 * (wid & 1) + (lane >> 4))) << 4));
  b.vx = (unsigned)((8 * wid + (lane >> 3)) * ROWB + (((lane & 7) ^ (4 * (wid & 1) + (wid & 2))) << 4));
  b.vw = b.vx ^ (lane & 16);
  b.db = (unsigned)(uintptr_t)lds + wid * 1024;
  return b;
}
#define HLA 2
__device__ __forceinline__ unsigned hsum(u32x4 a, u32x4 b, u32x4 c, u32x4 d, unsigned tag) {
  unsigned s = ((a.x + a.y + a.z) + (a.w + b.x + b.y) + (b.z + b.w + c.x)) + (c.y + c.z + c.w) + ((d.x + d.y + d.z) + d.w);
  s += (unsigned)__builtin_amdgcn_update_dpp(0, (int)s, 0x128, 0xf, 0xf, true);
  s += (unsigned)__builtin_amdgcn_update_dpp(0, (int)s, 0x124, 0xf, 0xf, true);
  s += (unsigned)__builtin_amdgcn_update_dpp(0, (int)s, 0x122, 0xf, 0xf, true);
  s += (unsigned)__builtin_amdgcn_update_dpp(0, (int)s, 0x121, 0xf, 0xf, true);
  return s + tag;
}
__device__ __forceinline__ void h_load(const char* src, unsigned l16, unsigned l4, u32x4 (&h)[4], unsigned& c) {
  asm volatile("global_load_dwordx4 %0, %5, %7 sc1\n\tglobal_load_dwordx4 %1, %5, %7 offset:1024 sc1\n\t"
               "global_load_dwordx4 %2, %5, %7 offset:2048 sc1\n\tglobal_load_dwordx4 %3, %5, %7 offset:3072 sc1\n\tglobal_load_dword %4, %6, %7 sc1"
               : "=&v"(h[0]), "=&v"(h[1]), "=&v"(h[2]), "=&v"(h[3]), "=&v"(c) : "v"(l16), "v"(l4), "s"(src) : "memory");
}
#define PINACC() asm volatile("" : "+v"(acc[0][0]), "+v"(acc[0][1]), "+v"(acc[0][2]), "+v"(acc[0][3]), "+v"(acc[1][0]), "+v"(acc[1][1]), "+v"(acc[1][2]), "+v"(acc[1][3]))
template <int PQ> __device__ __forceinline__ void b_own(f32x4 (&acc)[2][4], const bf16x8 (&cf)[2][4], const bf16x8 (&hf)[2][2], const f32x4 (&sk)[4]) {
  const f32x4 z4 = {0.f, 0.f, 0.f, 0.f};
  asm volatile("; own %0" ::"i"(PQ));
#pragma unroll
  for (int dt = 0; dt < 4; ++dt) { acc[1 - PQ][dt] = MFMA16(cf[0][dt], hf[0][1 - PQ], z4); acc[PQ][dt] = MFMA16(cf[0][dt], hf[0][PQ], sk[dt]); }
}
template <int PQ> __device__ __forceinline__ void b_tail(const f32x4 (&acc)[2][4], lptr mine, lptr other, const float* out, unsigned oo, lptr cnw, lptr cnp) {
  asm volatile("; tail %0" ::"i"(PQ));
#pragma unroll
  for (int dt = 0; dt < 4; ++dt) *(LAS a_f32x4*)(mine + dt * 1024) = acc[1 - PQ][dt];
  SB();
  *(LAS a_u32*)cnw = 3u;
  {
    unsigned spin = 0, v;
#pragma unroll 1
    do v = *(volatile LAS unsigned*)cnp; while (__builtin_amdgcn_ballot_w64(v != 3u) != 0 && ++spin < 2000000u);
  }
  SB();
  f32x4 o[4];
#pragma unroll
  for (int dt = 0; dt < 4; ++dt) o[dt] = *(LAS a_f32x4*)(other + dt * 1024);
  st16<0>(out, oo, o[0] + acc[PQ][0]); st16<64>(out, oo, o[1] + acc[PQ][1]);
  st16<128>(out, oo, o[2] + acc[PQ][2]); st16<192>(out, oo, o[3] + acc[PQ][3]);
}

__device__ __forceinline__ void partB(const Params& P, int chunk, int grp, lptr lds) {
  const int tid = threadIdx.x, lane = tid & 63, wid = __builtin_amdgcn_readfirstlane(tid >> 6);
  const int tq = wid >> 1, pq = wid & 1;
  const BSrc bs = b_src(P, chunk, grp, lds);
  unsigned lre, lim, ldt, epv, xa, wa;
  {
    const int r32 = lane & 31, wrow = pq * 32 + 8 * ((r32 >> 2) & 3) + 4 * (r32 >> 4) + (r32 & 3);
    const unsigned off = (unsigned)((grp * 64 + wrow) * 2);
    asm volatile("global_load_short_d16_hi %0, %1, %2" : "=v"(lre) : "v"(off), "s"(P.Lre), "0"(0u) : "memory");
    asm volatile("global_load_short_d16_hi %0, %1, %2" : "=v"(lim) : "v"(off), "s"(P.Lim), "0"(0u) : "memory");
    asm volatile("global_load_short_d16_hi %0, %1, %2" : "=v"(ldt) : "v"(off), "s"(P.ldt_b), "0"(0u) : "memory");
    asm volatile("global_load_dword %0, %1, %2 sc1" : "=v"(epv) : "v"(0u), "s"(P.ws + WS_EPOCH) : "memory");
    if (wid == 7) touch((const char*)P.dsk_b + grp * 128, (lane & 31) * 4, (unsigned)(uintptr_t)lds + B_SKIP);
    b_issue<1>(bs);
    const int g = (lane >> 2) & 3, i = lane & 3, g4 = lane >> 4;
    const int xrow = tq * 32 + 4 * (g & 1) + 16 * (g >> 1) + (i >> 1) + 8 * (i & 1), wr0 = pq * 32 + 8 * g + i;
    xa = (unsigned)(uintptr_t)lds + xrow * 128 + ((g4 ^ (4 * (i & 1) + (g & 2))) << 4);
    wa = (unsigned)(uintptr_t)lds + 16384 + wr0 * 128 + ((g4 ^ (4 * (g & 1) + (g & 2) + (i >> 1))) << 4);
  }
  const char* cr = (const char*)P.Cre + (size_t)grp * 64 * ROWB;
  const char* ci = (const char*)P.Cim + (size_t)grp * 64 * ROWB;
  unsigned tag = 0;
  float l2, rv, nr;
  C2 lamd, cfd, Q[8], W2;
  C1 lam, q, lam8, lam16, W7;
  bf16x8 S0, S1;
  unsigned gso, oo, l16;
  lptr exw, exr, e4, ca0, ca1, ca2, mine, other, cnw, cnr;
  float are[16], aim[16];
  u32x2 xv[4] = {};
  unsigned xb;
  {
    bf16x8 fx[2][2], fw[2][4];
    f32x4 ac[2][4] = {};
    lptr xp[2][2], wp[2][2];
#pragma unroll
    for (int kk = 0; kk < 2; ++kk) {
      xp[kk][0] = (lptr)(uintptr_t)(xa ^ (kk << 6)); wp[kk][0] = (lptr)(uintptr_t)(wa ^ (kk << 6));
      xp[kk][1] = xp[kk][0] + 65536; wp[kk][1] = wp[kk][0] + 65536;
      PIN4(xp[kk][0], xp[kk][1], wp[kk][0], wp[kk][1]);
    }
    unsigned vx = bs.vx, vw = bs.vw, cv[4];
#pragma unroll
    for (int s = 0; s < 9; ++s) {
      SB();
      if (s > 0) __builtin_amdgcn_s_waitcnt(0xc07f);
      SB();
      if (s == 0) {
        unsigned ep;
        asm volatile("s_waitcnt vmcnt(4)\n\ts_barrier\n\tv_readfirstlane_b32 %0, %4" : "=s"(ep), "+v"(lre), "+v"(lim), "+v"(ldt) : "v"(epv) : "memory");
        tag = ep + 1u;
      }
      else waitvm_bar<4 * GLA - 4>();
      auto consts = [&](int h) __attribute__((always_inline)) {
        if (h == 0) {
          int ln = lane;
          PIN1(ln);
          const int hh = ln >> 5, run = 2 * tq + hh, pl = pq * 32 + (ln & 31);
          exw = lds + B_EX + (run * 64 + pl) * 8; exr = lds + B_EX + pl * 8; e4 = exr + 4096 + hh * 2048;
          gso = (unsigned)((chunk * 64 + pl) * 16); l16 = ln * 16; nr = (float)(16 * run);
          const float dt = rnd_bf16(exp_acc(asf(ldt))), zr = dt * asf(lre);
          l2 = zr * LOG2E; rv = (dt * asf(lim)) * INV2PI;
          const float m = exp_acc(zr), th = __builtin_amdgcn_fractf(rv);
          lam = C1{m * __builtin_amdgcn_cosf(th), m * __builtin_amdgcn_sinf(th)};
          lamd = dup(lam);
          lam8 = cexp1(8.f * l2, 8.f * rv);
          W7 = cexp1((112.f - nr) * l2, (112.f - nr) * rv);
          const int n16 = ln & 15, g4 = ln >> 4;
          xb = (unsigned)(uintptr_t)lds + (tq * 32 + 16 * (n16 >> 3) + 4 * pq + ((n16 & 7) >> 1) + 8 * (n16 & 1)) * 128 + (n16 & 1) * 64 + (n16 >> 3) * 32 + g4 * 8;
          PIN1(xb);
        }
        if (h == 11) { cv[0] = c_voff(grp, 8, bs.voff); cv[1] = c_voff(grp, 2, bs.voff); }
        if (h == 13) { cv[2] = c_voff(grp, 3, bs.voff); cv[3] = c_voff(grp, 4, bs.voff); }
        if (h == 15) { cv[0] = c_voff(grp, 5, bs.voff); cv[1] = c_voff(grp, 6, bs.voff); }
      };
      auto pins = [&](int h) __attribute__((always_inline)) {
        SB();
        if (h == 0) { PIN4(exw, exr, gso, l16); PIN4(e4, nr, l2, rv); PIN4(lam.r, lam.i, lamd.r, lamd.i); PIN4(lam8.r, lam8.i, W7.r, W7.i); }
        if (h == 2 * (4 - GLA) - 1 || h == 2 * (8 - GLA) - 1) { PIN2(vx, vw); vx += 512; vw += 512; PIN2(vx, vw); }
        if (h == 11 || (h == 15)) PIN2(cv[0], cv[1]);
        if (h == 13) PIN2(cv[2], cv[3]);
        if ((h & 1) == 0) {
          unsigned t0, t1, t2, t3;
          asm volatile("s_cmp_lg_u32 %9, %10\n\ts_cbranch_scc1 1f\n\tv_xor_b32 %4, %11, %8\n\tv_xor_b32 %5, %12, %8\n\tv_xor_b32 %6, %13, %8\n\tv_xor_b32 %7, %14, %8\n\t"
                       "ds_read_b64 %0, %4 offset:%15\n\tds_read_b64 %1, %5 offset:%15\n\tds_read_b64 %2, %6 offset:%15\n\tds_read_b64 %3, %7 offset:%15\n1:"
                       : "+v"(xv[0]), "+v"(xv[1]), "+v"(xv[2]), "+v"(xv[3]), "=&v"(t0), "=&v"(t1), "=&v"(t2), "=&v"(t3)
                       : "v"(xb), "s"(grp), "i"(h >> 1), "i"((h & 4) << 14), "i"(((h & 4) << 14) + 32), "i"(((h & 4) << 14) + 64), "i"(((h & 4) << 14) + 96), "i"((h & 2) << 14)
                       : "memory", "scc");
        }
        SB();
      };
      auto fetch = [&](int n) __attribute__((always_inline)) {
#define B_PC(S) if (s + GLA == S) { if (n == 0) b_piece<S, 0>(bs, vx, vw); if (n == 1) b_piece<S, 1>(bs, vx, vw); if (n == 2) b_piece<S, 2>(bs, vx, vw); if (n == 3) b_piece<S, 3>(bs, vx, vw); }
        if (s == 0) {
          if (n == 0) { b_piece<2, 0>(bs, vx, vw); b_piece<2, 1>(bs, vx, vw); } if (n == 1) { b_piece<2, 2>(bs, vx, vw); b_piece<2, 3>(bs, vx, vw); }
          if (n == 2) { b_piece<3, 0>(bs, vx, vw); b_piece<3, 1>(bs, vx, vw); } if (n == 3) { b_piece<3, 2>(bs, vx, vw); b_piece<3, 3>(bs, vx, vw); }
        } else { B_PC(2) B_PC(3) B_PC(4) B_PC(5) B_PC(6) B_PC(7) B_PC(8) }
#define C_PC(T, A, B, I) if (s + GLA == T) { if (n == 0) glds16o<A * 16384, 0>(cr, cv[I], bs.db); if (n == 1) glds16o<A * 16384 + 8192, 0>(ci, cv[I], bs.db); \
          if (n == 2) glds16o<B * 16384, 0>(cr, cv[I + 1], bs.db); if (n == 3) glds16o<B * 16384 + 8192, 0>(ci, cv[I + 1], bs.db); }
        C_PC(9, 8, 2, 0) C_PC(10, 3, 4, 2) C_PC(11, 5, 6, 0)
      };
      const int up = (s >> 1) & 1, so = (s & 1) * 32768;
#pragma unroll
      for (int kk = 0; kk < 2; ++kk) {
        const bool on = s > 0 || kk == 1;
        SB();
        if (kk == 1) __builtin_amdgcn_s_waitcnt(0xc07f);
        SB();
        consts(2 * s + kk);
        bf16x8 nx[2], nw[4];
#pragma unroll
        for (int j = 0; j < 8; ++j) {
          if (on) ac[j >> 2][j & 3] = MFMA16(fx[kk ^ 1][j >> 2], fw[kk ^ 1][j & 3], ac[j >> 2][j & 3]);
          if (j < 2) nx[j] = lds128(xp[kk][up] + so + j * 256);
          else if (j < 6) nw[j - 2] = lds128(wp[kk][up] + so + (j & 1) * 8192 + ((j - 2) >> 1) * 512);
          else fetch(2 * kk + j - 6);
        }
#pragma unroll
        for (int j = 0; j < 4; ++j) { if (j < 2) fx[kk][j] = nx[j]; fw[kk][j] = nw[j]; }
#pragma unroll
        for (int i = 0; i < 8; ++i) {
          if (on) __builtin_amdgcn_sched_group_barrier(0x8, 1, 0);
          if (i < 6) __builtin_amdgcn_sched_group_barrier(0x100, 1, 0);
          __builtin_amdgcn_sched_group_barrier(0x402, NV2, 0);
        }
        pins(2 * s + kk);
      }
    }
    __builtin_amdgcn_s_waitcnt(0xc07f);
    SB();
#pragma unroll
    for (int rt = 0; rt < 2; ++rt)
#pragma unroll
      for (int c = 0; c < 4; ++c) ac[rt][c] = MFMA16(fx[1][rt], fw[1][c], ac[rt][c]);
#pragma unroll
    for (int rt = 0; rt < 2; ++rt)
#pragma unroll
      for (int c = 0; c < 2; ++c)
#pragma unroll
        for (int i = 0; i < 4; ++i) {
          const auto r = __builtin_amdgcn_permlane16_swap(asu(ac[rt][c][i]), asu(ac[rt][c + 2][i]), false, false);
          const unsigned r0 = r[0], r1 = r[1];
          if (c == 0) { are[4 * rt + i] = asf(r0); are[8 + 4 * rt + i] = asf(r1); } else { aim[4 * rt + i] = asf(r0); aim[8 + 4 * rt + i] = asf(r1); }
        }
  }
  const int hh = lane >> 5;
  const char* gb = P.ws + WS_GRAN + grp * 16384;
  const int hset = (chunk * 4 + tq) * 2 + pq;
  const char* hb = P.ws + WS_H + (size_t)hset * (9 * H_BLK);
  f32x2 gr[8], gi[8];
  gr[0] = f32x2{are[0], are[1]}; gi[0] = f32x2{aim[0], aim[1]};
#pragma unroll
  for (int k = 1; k < 8; ++k) {
    const f32x2 re = {are[2 * k], are[2 * k + 1]}, im = {aim[2 * k], aim[2 * k + 1]};
    gr[k] = PFMA(-lamd.i, gi[k - 1], PFMA(lamd.r, gr[k - 1], re));
    gi[k] = PFMA(lamd.i, gr[k - 1], PFMA(lamd.r, gi[k - 1], im));
  }
  const C1 sA{gr[7].x, gi[7].x};
  {
    const C1 s = cfma(lam8, sA, C1{gr[7].y, gi[7].y}), sw = cmul(s, W7);
    *(LAS a_f32x2*)(exw) = f32x2{s.r, s.i};
    *(LAS a_f32x2*)(exw + 4096) = f32x2{sw.r, sw.i};
  }
  SB();
  BAR();
  auto rd = [&](int o) { f32x2 v = *(LAS a_f32x2*)(exr + o); return C1{v.x, v.y}; };
  C1 lp{0.f, 0.f};
  if (tq == 0) {
    const f32x2 a = *(LAS a_f32x2*)(e4), b = *(LAS a_f32x2*)(e4 + 512), c = *(LAS a_f32x2*)(e4 + 1024), d = *(LAS a_f32x2*)(e4 + 1536);
    const C1 s0 = rd(0);
    const f32x2 t = (a + b) + (c + d);
    const float tx = t.x, ty = t.y;
    const auto xr = __builtin_amdgcn_permlane32_swap(asu(tx), asu(tx), false, false);
    const auto xi = __builtin_amdgcn_permlane32_swap(asu(ty), asu(ty), false, false);
    const unsigned r0 = xr[0], r1 = xr[1], i0 = xi[0], i1 = xi[1];
    const u32x4 g = {asu(asf(r0) + asf(r1)), tag, asu(asf(i0) + asf(i1)), tag};
    if (chunk < 15)
      asm volatile("s_mov_b32 exec_hi, 0\n\tglobal_store_dwordx4 %0, %1, %2 sc1\n\ts_mov_b32 exec_hi, -1\n\ts_nop 0" ::"v"(gso), "v"(g), "s"(gb) : "memory");
    if (hh) lp = s0;
  }
  SB();
  if (wid >= 4) {
    const unsigned v2 = bs.voff - 32 * ROWB, d2 = bs.db - 4096;
    c_issue<0>(cr, ci, grp, bs.voff, bs.db); c_issue<0>(cr, ci, grp, v2, d2); c_issue<1>(cr, ci, grp, bs.voff, bs.db); c_issue<1>(cr, ci, grp, v2, d2);
    c_issue<7>(cr, ci, grp, bs.voff, bs.db); c_issue<7>(cr, ci, grp, v2, d2);
  }
  SB();
  lam16 = cexp1(16.f * l2, 16.f * rv);
  {
    const float inv = 2.f * __builtin_amdgcn_rcpf(asf(lre) * asf(lre) + asf(lim) * asf(lim));
    q = cmul(C1{lam.r - 1.f, lam.i}, C1{asf(lre) * inv, -asf(lim) * inv});
    cfd = dup(q);
    const C1 a = cexp1(nr * l2, nr * rv), b = cmul(a, lam8);
    W2 = C2{f32x2{a.r, b.r}, f32x2{a.i, b.i}};
  }
  if (tq != 0) {
    lp = cfma(lam16, rd(0), rd(512));
    for (int r = 2; r < 2 * tq; r += 2) lp = cfma(lam16, cfma(lam16, lp, rd(r * 512)), rd(r * 512 + 512));
    const C1 e = cfma(lam16, lp, rd(2 * tq * 512));
    if (hh) lp = e;
  }
  C2 lp2;
  {
    const C1 e = cfma(lam8, lp, sA);
    lp2 = C2{f32x2{lp.r, e.r}, f32x2{lp.i, e.i}};
#pragma unroll
    for (int k = 0; k < 8; ++k) {
      const f32x2 a = PFMA(-cfd.i, gi[k], cfd.r * gr[k]), b = PFMA(-cfd.i, gr[k], -cfd.r * gi[k]);
      gr[k] = a; gi[k] = b;
    }
    asm volatile("" : "+v"(gr[0]), "+v"(gr[1]), "+v"(gr[2]), "+v"(gr[3]), "+v"(gr[4]), "+v"(gr[5]), "+v"(gr[6]), "+v"(gr[7]),
                 "+v"(gi[0]), "+v"(gi[1]), "+v"(gi[2]), "+v"(gi[3]), "+v"(gi[4]), "+v"(gi[5]), "+v"(gi[6]), "+v"(gi[7]), "+v"(lp2.r), "+v"(lp2.i));
  }
  {
    {
      int ln = lane;
      PIN1(ln);
      const int n16 = ln & 15, g4 = ln >> 4;
#pragma unroll
      for (int k = 0; k < 8; ++k) { q = cmul(q, lam); Q[k] = dup(q); }
      const int trow = chunk * 128 + tq * 32 + 16 * (n16 >> 3) + 4 * pq + ((n16 & 7) >> 1) + 8 * (n16 & 1), dcol = grp * 64 + 4 * g4;
      oo = (unsigned)(trow * (8 * D) + (D + dcol) * 4);
      ca0 = lds + n16 * 128 + (((pq * 4 + g4) ^ (n16 >> 1)) << 4); ca1 = ca0 + 65536; ca2 = ca0 + 131072;
      mine = lds + (wid < 4 ? 131072 : -16384) + wid * 4096 + l16;
      other = lds + (wid < 4 ? 131072 : -16384) + (wid ^ 1) * 4096 + l16;
      cnw = lds + B_CNT + wid * 256 + ln * 4; cnr = lds + B_CNT + (ln & 7) * 260;
      *(LAS a_u32*)cnw = 0u;
      const unsigned one = (n16 & 1) ? 0x3f800000u : 0x3f80u;
      const int w = (n16 & 7) >> 1, gs = g4 - 2 * (n16 >> 3);
      const unsigned a = gs == 0 ? one : 0u, b = gs == 1 ? one : 0u;
      S0 = cat4(w == 0 ? a : 0u, w == 1 ? a : 0u, w == 2 ? a : 0u, w == 3 ? a : 0u);
      S1 = cat4(w == 0 ? b : 0u, w == 1 ? b : 0u, w == 2 ? b : 0u, w == 3 ? b : 0u);
      PIN4(Q[0].r, Q[0].i, Q[1].r, Q[1].i); PIN4(Q[2].r, Q[2].i, Q[3].r, Q[3].i); PIN4(Q[4].r, Q[4].i, Q[5].r, Q[5].i); PIN4(Q[6].r, Q[6].i, Q[7].r, Q[7].i);
      PIN2(oo, ca0); PIN4(ca1, ca2, mine, other); PIN4(cnw, cnr, S0, S1);
    }
  }
  PIN2(W2.r, W2.i);
  SB();
  if (wid < 4) {
    f32x2 part = {0.f, 0.f};
    if (8 * tq < chunk) {
      const int c0 = 8 * tq + hh;
      const unsigned go = gso + (unsigned)((c0 - chunk) * 1024), go2 = go + 4096;
      C1 gw[4];
      u64 mk[4];
      u32x4 g[4];
#pragma unroll
      for (int j = 0; j < 4; ++j) {
        const float n = (float)(128 * max(chunk - 1 - c0 - 2 * j, 0));
        gw[j] = cexp1(n * l2, n * rv);
        mk[j] = __builtin_amdgcn_ballot_w64(c0 + 2 * j < chunk);
        g[j] = u32x4{0u, tag, 0u, tag};
      }
      PIN4(gw[0].r, gw[0].i, gw[1].r, gw[1].i); PIN4(gw[2].r, gw[2].i, gw[3].r, gw[3].i);
      unsigned spin = 0, bad;
#pragma unroll 1
      do {
        asm volatile("s_mov_b64 exec, %7\n\tglobal_load_dwordx4 %0, %4, %6 sc1\n\ts_mov_b64 exec, %8\n\tglobal_load_dwordx4 %1, %4, %6 offset:2048 sc1\n\t"
                     "s_mov_b64 exec, %9\n\tglobal_load_dwordx4 %2, %5, %6 sc1\n\ts_mov_b64 exec, %10\n\tglobal_load_dwordx4 %3, %5, %6 offset:2048 sc1\n\t"
                     "s_mov_b64 exec, -1\n\ts_waitcnt vmcnt(0)"
                     : "+v"(g[0]), "+v"(g[1]), "+v"(g[2]), "+v"(g[3]) : "v"(go), "v"(go2), "s"(gb), "s"(mk[0]), "s"(mk[1]), "s"(mk[2]), "s"(mk[3]) : "memory");
        bad = (((g[0].y ^ tag) | (g[0].w ^ tag)) | ((g[1].y ^ tag) | (g[1].w ^ tag))) | (((g[2].y ^ tag) | (g[2].w ^ tag)) | ((g[3].y ^ tag) | (g[3].w ^ tag)));
      } while (__builtin_amdgcn_ballot_w64(bad != 0u) != 0 && ++spin < 20000u);
      const C1 t0 = cfma(C1{asf(g[1].x), asf(g[1].z)}, gw[1], cmul(C1{asf(g[0].x), asf(g[0].z)}, gw[0]));
      const C1 t1 = cfma(C1{asf(g[3].x), asf(g[3].z)}, gw[3], cmul(C1{asf(g[2].x), asf(g[2].z)}, gw[2]));
      part = f32x2{t0.r + t1.r, t0.i + t1.i};
    }
    *(LAS a_f32x2*)(exw + 8192) = part;
    WAITV(0);
  } else WAITV(4);
  SB();
  BAR();
  bf16x8 hf[2][2];
  {
    f32x2 Hp[4];
#pragma unroll
    for (int w = 0; w < 4; ++w) Hp[w] = *(LAS a_f32x2*)(exr + 8192 + w * 512);
    const f32x2 H = (Hp[0] + Hp[1]) + (Hp[2] + Hp[3]);
    const f32x2 Hr = dup(H.x), Hi = dup(H.y);
    const f32x2 Er = PFMA(-W2.i, Hi, PFMA(W2.r, Hr, lp2.r)), Ei = PFMA(W2.i, Hr, PFMA(W2.r, Hi, lp2.i));
    const f32x4 z4 = {0.f, 0.f, 0.f, 0.f};
#pragma unroll
    for (int ts = 0; ts < 2; ++ts) {
      unsigned hr[4], hi[4];
#pragma unroll
      for (int k4 = 0; k4 < 4; ++k4) {
        const int k = 4 * ts + k4;
        const f32x2 a = PFMA(-Q[k].i, Ei, PFMA(Q[k].r, Er, gr[k])), b = PFMA(-Q[k].i, Er, PFMA(-Q[k].r, Ei, gi[k]));
        hr[k4] = cvtpk(a.x, a.y); hi[k4] = cvtpk(b.x, b.y);
      }
      const bf16x8 ar = cat4(hr[0], hr[1], hr[2], hr[3]), ai = cat4(hi[0], hi[1], hi[2], hi[3]);
      const f32x4 d0 = MFMA16(ar, S0, z4), d1 = MFMA16(ar, S1, z4), d2 = MFMA16(ai, S0, z4), d3 = MFMA16(ai, S1, z4);
      hf[0][ts] = cat4(cvtpk(d0[0], d0[1]), cvtpk(d0[2], d0[3]), cvtpk(d1[0], d1[1]), cvtpk(d1[2], d1[3]));
      hf[1][ts] = cat4(cvtpk(d2[0], d2[1]), cvtpk(d2[2], d2[3]), cvtpk(d3[0], d3[1]), cvtpk(d3[2], d3[3]));
    }
  }
  const char* hown = hb + grp * H_BLK;
  unsigned l4 = (lane >> 4) * 4 + 4096;
  PIN1(l4);
  st16<0>(hown, l16, hf[0][0]); st16<1024>(hown, l16, hf[0][1]); st16<2048>(hown, l16, hf[1][0]); st16<3072>(hown, l16, hf[1][1]);
  {
    const unsigned cs = hsum(__builtin_bit_cast(u32x4, hf[0][0]), __builtin_bit_cast(u32x4, hf[0][1]), __builtin_bit_cast(u32x4, hf[1][0]), __builtin_bit_cast(u32x4, hf[1][1]), tag);
    asm volatile("s_mov_b32 exec_lo, 0x10001\n\ts_mov_b32 exec_hi, 0x10001\n\tglobal_store_dword %0, %1, %2 sc1\n\ts_mov_b64 exec, -1" ::"v"(l4), "v"(cs), "s"(hown) : "memory");
  }
  if (wid >= 4) __builtin_amdgcn_s_setprio(1);
  f32x4 acc[2][4];
  const char* hs[8];
  {
#pragma unroll
    for (int k = 0; k < 8; ++k) { int j = grp + 1 + k; if (j >= 9) j -= 9; hs[k] = hb + j * H_BLK; asm volatile("" : "+s"(hs[k])); }
    WAITV(0);
    __builtin_amdgcn_s_sleep(8);
  }
  {
    u32x4 hv[8][4];
    unsigned cw[8];
#pragma unroll
    for (int k = 0; k < HLA; ++k) h_load(hs[k], l16, l4, hv[k], cw[k]);
    {
      f32x4 sk[4];
      bf16x8 cf[2][4];
#pragma unroll
      for (int c = 0; c < 2; ++c)
#pragma unroll
        for (int dt = 0; dt < 4; ++dt) cf[c][dt] = lds128(ca2 + c * 8192 + dt * 2048);
      int ln = lane;
      PIN1(ln);
      const lptr dp = lds + B_SKIP + (ln >> 4) * 8;
#pragma unroll
      for (int dt = 0; dt < 4; ++dt) {
        const u32x2 d = *(LAS a_u32x2*)(dp + dt * 32);
        const f32x2 a = f32x2{bflo(xv[dt].x), bfhi(xv[dt].x)} * f32x2{bflo(d.x), bfhi(d.x)};
        const f32x2 b = f32x2{bflo(xv[dt].y), bfhi(xv[dt].y)} * f32x2{bflo(d.y), bfhi(d.y)};
        sk[dt] = f32x4{a.x, a.y, b.x, b.y};
      }
      PIN4(sk[0], sk[1], sk[2], sk[3]);
      if (pq) b_own<1>(acc, cf, hf, sk); else b_own<0>(acc, cf, hf, sk);
#pragma unroll
      for (int dt = 0; dt < 4; ++dt) { acc[0][dt] = MFMA16(cf[1][dt], hf[1][0], acc[0][dt]); acc[1][dt] = MFMA16(cf[1][dt], hf[1][1], acc[1][dt]); }
      PINACC();
      SB();
    }
    bf16x8 cf[2][4];
#pragma unroll
    for (int dt = 0; dt < 4; ++dt) cf[0][dt] = lds128(ca0 + dt * 2048);
    unsigned seen = 0;
    auto check = [&](int k) {
      SB();
      asm volatile("s_waitcnt vmcnt(%5)" : "+v"(hv[k][0]), "+v"(hv[k][1]), "+v"(hv[k][2]), "+v"(hv[k][3]), "+v"(cw[k]) : "i"(5 * (HLA - 1 < 7 - k ? HLA - 1 : 7 - k)) : "memory");
      return hsum(hv[k][0], hv[k][1], hv[k][2], hv[k][3], tag);
    };
    auto fix = [&](int k, unsigned cs) {
      PINACC();
      if (__builtin_expect(__builtin_amdgcn_ballot_w64(cs != cw[k]) != 0, 0)) {
        unsigned spin = 0;
#pragma unroll 1
        do {
          asm volatile("global_load_dwordx4 %0, %5, %7 sc1\n\tglobal_load_dwordx4 %1, %5, %7 offset:1024 sc1\n\t"
                       "global_load_dwordx4 %2, %5, %7 offset:2048 sc1\n\tglobal_load_dwordx4 %3, %5, %7 offset:3072 sc1\n\t"
                       "global_load_dword %4, %6, %7 sc1\n\ts_waitcnt vmcnt(0)"
                       : "+v"(hv[k][0]), "+v"(hv[k][1]), "+v"(hv[k][2]), "+v"(hv[k][3]), "+v"(cw[k]) : "v"(l16), "v"(l4), "s"(hs[k]) : "memory");
        } while (__builtin_amdgcn_ballot_w64(hsum(hv[k][0], hv[k][1], hv[k][2], hv[k][3], tag) != cw[k]) != 0 && ++spin < 20000u);
      }
    };
    fix(0, check(0));
#pragma unroll
    for (int un = 0; un < 16; ++un) {
      SB();
      if (un == 2) *(LAS a_u32*)cnw = 1u;
      if (un == 9) seen = *(volatile LAS unsigned*)cnr;
      if (un == 11) {
        PINACC();
        unsigned spin = 0;
#pragma unroll 1
        while (__builtin_amdgcn_ballot_w64(seen == 0u) != 0 && ++spin < 200000u) seen = *(volatile LAS unsigned*)cnr;
        SB();
      }
      const int k = un >> 1, n = k + HLA, o = (un + 1) * 8192;
      lptr cb = o < 65536 ? ca0 + o : ca1 + (o - 65536);
      const bf16x8 h0 = __builtin_bit_cast(bf16x8, hv[k][2 * (un & 1)]), h1 = __builtin_bit_cast(bf16x8, hv[k][2 * (un & 1) + 1]);
      unsigned cs = 0;
#pragma unroll
      for (int dt = 0; dt < 4; ++dt) {
        acc[0][dt] = MFMA16(cf[un & 1][dt], h0, acc[0][dt]);
        if (un < 15) cf[(un + 1) & 1][dt] = lds128(cb + dt * 2048);
        SB();
        if ((un & 1) == 0 && n < 8) {
          if (dt == 0) asm volatile("global_load_dwordx4 %0, %1, %2 sc1" : "=v"(hv[n][0]) : "v"(l16), "s"(hs[n]) : "memory");
          if (dt == 1) asm volatile("global_load_dwordx4 %0, %1, %2 offset:1024 sc1" : "=v"(hv[n][1]) : "v"(l16), "s"(hs[n]) : "memory");
          if (dt == 2) asm volatile("global_load_dwordx4 %0, %1, %2 offset:2048 sc1" : "=v"(hv[n][2]) : "v"(l16), "s"(hs[n]) : "memory");
          if (dt == 3) asm volatile("global_load_dwordx4 %0, %1, %2 offset:3072 sc1" : "=v"(hv[n][3]) : "v"(l16), "s"(hs[n]) : "memory");
        }
        if ((un & 1) == 1 && dt == 0 && n < 8) asm volatile("global_load_dword %0, %1, %2 sc1" : "=v"(cw[n]) : "v"(l4), "s"(hs[n]) : "memory");
        if ((un & 1) == 1 && dt == 2 && k < 7) cs = check(k + 1);
        acc[1][dt] = MFMA16(cf[un & 1][dt], h1, acc[1][dt]);
        if (!((un & 1) == 1 && dt >= 2)) SB();
      }
      if ((un & 1) == 1 && k < 7) {
        __builtin_amdgcn_sched_group_barrier(0x8, 1, 0); __builtin_amdgcn_sched_group_barrier(0x2, 3, 0);
        __builtin_amdgcn_sched_group_barrier(0x8, 1, 0); __builtin_amdgcn_sched_group_barrier(0x102, 4, 0);
        __builtin_amdgcn_sched_group_barrier(0x8, 1, 0); __builtin_amdgcn_sched_group_barrier(0x2, 3, 0);
        SB();
        fix(k + 1, cs);
      }
    }
    PINACC();
    SB();
  }
  PINACC();
  SB();
  if (pq) b_tail<1>(acc, mine, other, P.out, oo, cnw, cnw - 256); else b_tail<0>(acc, mine, other, P.out, oo, cnw, cnw + 256);
  if (chunk == 15 && grp == 0 && wid == 0)
    asm volatile("s_mov_b64 exec, 1\n\tglobal_store_dword %0, %1, %2 sc1\n\ts_mov_b64 exec, -1" ::"v"(0u), "v"(tag), "s"(P.ws + WS_EPOCH) : "memory");
}

constexpr int CODE_BYTES = 28416;

__global__ __launch_bounds__(512, 2) void fused(Params Pk) {
  extern __shared__ __attribute__((aligned(16))) char smem[];
  lptr lds = (lptr)smem;
  const int xcd = blockIdx.x & 7, loc = blockIdx.x >> 3;
  u64 code;
  asm volatile("s_getpc_b64 %0" : "=s"(code));
  const __attribute__((address_space(4))) Params* pp = (const __attribute__((address_space(4))) Params*)__builtin_amdgcn_kernarg_segment_ptr();
  asm volatile("" : "+s"(pp));
  Params P;
  P.u = pp->u; P.Are = pp->Are; P.Aim = pp->Aim; P.Bre_a = pp->Bre_a; P.Bim_a = pp->Bim_a; P.Cre_a = pp->Cre_a; P.Cim_a = pp->Cim_a;
  P.ldt_a = pp->ldt_a; P.dsk_a = pp->dsk_a; P.x = pp->x; P.Lre = pp->Lre; P.Lim = pp->Lim; P.Bre = pp->Bre; P.Bim = pp->Bim;
  P.Cre = pp->Cre; P.Cim = pp->Cim; P.ldt_b = pp->ldt_b; P.dsk_b = pp->dsk_b; P.out = pp->out; P.ws = pp->ws;
  const int lane = threadIdx.x & 63, wid = __builtin_amdgcn_readfirstlane(threadIdx.x >> 6);
  const unsigned lds0 = (unsigned)(uintptr_t)lds;
  const int na = 8 + 2 * (xcd & 1), nh = 112 - 8 * na;
  const int chunk = 2 * xcd + (loc >= 10), grp = loc >= 10 ? loc - 10 : loc - 1, cg0 = 18 * (xcd >> 1) + 8 * (xcd & 1), cg = cg0 + loc - 19;
  if (loc == 0 || loc >= 19 + na) {
    if (loc == 0 && wid < 4) touch((const char*)(code & ~(u64)255), min(wid * 8192 + lane * 128, CODE_BYTES - 4), lds0 + wid * 1024);
  } else if (loc >= 19) {
    const unsigned o = (unsigned)((cg * 8 + wid) * 256 + lane * 4), dst = lds0 + wid * A_REG;
    touch(P.Are, o, dst); touch(P.Aim, o, dst + 256); touch(P.Bre_a, o, dst + 512);
    touch(P.Bim_a, o, dst + 768); touch(P.Cre_a, o, dst + 1024); touch(P.Cim_a, o, dst + 1280);
  } else {
    b_issue<0>(b_src(P, chunk, grp, lds));
    asm volatile("s_barrier" ::: "memory");
  }
  int role = loc;
  asm volatile("" : "+s"(role));
  if (role == 0 || role >= 19 + na) {
    const int h = (role == 0 ? 0 : role - 18 - na) * 8 + wid;
    const unsigned dst = lds0 + wid * 1024;
    b_touch(P, xcd, h, lane, dst);
    WAITV(0);
    if (h < 42) b_touch(P, xcd, 112 + h, lane, dst);
    if (h + nh < 42) b_touch(P, xcd, 112 + nh + h, lane, dst);
    WAITV(0);
    const char* ub = (const char*)P.u + (cg0 >> 3) * 128;
    auto unit = [&](int q) __attribute__((always_inline)) {
      const int c = min(xcd * 21 + q, 161);
      const char* sc = (const char*)(c < 81 ? P.Cre : P.Cim) + (c < 81 ? c : c - 81) * 8192;
      const char* su = ub + ((q - 21) >> 5) * 128 + ((q - 21) & 31) * 64 * ROWB;
      if (q < 85) touch(q < 21 ? sc : su, lane * (q < 21 ? 128 : ROWB), dst);
    };
    unit(h);
    WAITV(0);
    unit(h + nh); unit(h + 2 * nh);
    WAITV(0);

    __builtin_amdgcn_endpgm();
  }
  if (role >= 19) { partA(P, cg, nh + (loc - 19) * 8, lds); __builtin_amdgcn_endpgm(); }
  partB(P, chunk, grp, lds);
}

extern "C" void kernel_launch(void* const* d_in, const int* in_sizes, int n_in, void* d_out, int out_size, void* d_ws,
                              size_t ws_size, hipStream_t stream) {
  Params P;
  P.u = (const u16*)d_in[0]; P.Are = (const u16*)d_in[1]; P.Aim = (const u16*)d_in[2]; P.Bre_a = (const u16*)d_in[3];
  P.Bim_a = (const u16*)d_in[4]; P.Cre_a = (const u16*)d_in[5]; P.Cim_a = (const u16*)d_in[6]; P.ldt_a = (const u16*)d_in[7];
  P.dsk_a = (const u16*)d_in[8]; P.x = (const u16*)d_in[9]; P.Lre = (const u16*)d_in[10]; P.Lim = (const u16*)d_in[11];
  P.Bre = (const u16*)d_in[12]; P.Bim = (const u16*)d_in[13]; P.Cre = (const u16*)d_in[14]; P.Cim = (const u16*)d_in[15];
  P.ldt_b = (const u16*)d_in[16]; P.dsk_b = (const u16*)d_in[17];
  P.out = (float*)d_out; P.ws = (char*)d_ws;
  (void)hipFuncSetAttribute((const void*)fused, hipFuncAttributeMaxDynamicSharedMemorySize, 163840);
  fused<<<256, 512, 163840, stream>>>(P);
}
```
